```python
import torch

def _softmax(x, axis=-1):
    return torch.softmax(x, dim=axis)
def _gelu(x):
    return torch.nn.functional.gelu(x, approximate="tanh")
def _silu(x):
    return torch.nn.functional.silu(x)
def _sigmoid(x):
    return torch.sigmoid(x)
def _layernorm(x, axis=-1, eps=1e-5):
    m = x.mean(dim=axis, keepdim=True)
    v = x.var(dim=axis, keepdim=True, unbiased=False)
    return (x - m) / torch.sqrt(v + eps)
def _scatter_add(tgt, idx, src, axis=0):
    return tgt.clone().index_add_(axis, idx, src)
def _segment_sum(src, seg, n):
    out = torch.zeros((n,) + src.shape[1:], dtype=src.dtype)
    return out.index_add_(0, seg, src)
def _pad(x, pw):
    flat = tuple(v for pair in reversed(pw) for v in pair)
    return torch.nn.functional.pad(x, flat)

kernel_name = 'concat_heads__gqa_block_cached__mha_block_cached__near_ridge'

def setup_inputs():

    _w = (lambda a: (lambda t: t.to(torch.bfloat16) if t.is_floating_point() else t)(torch.as_tensor(a)))
    def _A_setup():
        B, Sq, D = 8, 80, 512
        Hq, Hkv, Sk, Dh = 32, 8, 1024, 128
        assert Hq % Hkv == 0, (Hq, Hkv)
        return {
            "x": _w((lambda *s: torch.randn(*s, dtype=torch.bfloat16) * 0.8)(B, Sq, D)),
            "k_cache": _w((lambda *s: torch.randn(*s, dtype=torch.bfloat16) * 0.8)(B, Hkv, Sk, Dh)),
            "v_cache": _w((lambda *s: torch.randn(*s, dtype=torch.bfloat16) * 0.05)(B, Hkv, Sk, Dh)),
            "wq": _w((lambda *s: torch.randn(*s, dtype=torch.bfloat16) * 0.8)(D, Hq * Dh) * (D ** -0.5)),
            "wo": _w((lambda *s: torch.randn(*s, dtype=torch.bfloat16) * 0.05)(Hq * Dh, D) * ((Hq * Dh) ** -0.5)),
        }
    _A_in = _A_setup()

    def _B_setup():
        B, Sq, D = 8, 80, 512
        H, Sk, Dh = 32, 1024, 128
        return {
            "x": _w((lambda *s: torch.randn(*s, dtype=torch.bfloat16) * 0.8)(B, Sq, D)),
            "k_cache": _w((lambda *s: torch.randn(*s, dtype=torch.bfloat16) * 0.8)(B, H, Sk, Dh)),
            "v_cache": _w((lambda *s: torch.randn(*s, dtype=torch.bfloat16) * 0.05)(B, H, Sk, Dh)),
            "wq": _w((lambda *s: torch.randn(*s, dtype=torch.bfloat16) * 0.8)(D, H * Dh) * (D ** -0.5)),
            "wo": _w((lambda *s: torch.randn(*s, dtype=torch.bfloat16) * 0.05)(H * Dh, D) * ((H * Dh) ** -0.5)),
        }
    _B_in = _B_setup()

    return {
        "A_x": _A_in["x"],
        "A_k_cache": _A_in["k_cache"],
        "A_v_cache": _A_in["v_cache"],
        "A_wq": _A_in["wq"],
        "A_wo": _A_in["wo"],
        "B_x": _B_in["x"],
        "B_k_cache": _B_in["k_cache"],
        "B_v_cache": _B_in["v_cache"],
        "B_wq": _B_in["wq"],
        "B_wo": _B_in["wo"],
    }

def reference(A_x, A_k_cache, A_v_cache, A_wq, A_wo, B_x, B_k_cache, B_v_cache, B_wq, B_wo):
    def _A_ref(x, k_cache, v_cache, wq, wo):
        B, Sq, D = x.shape
        Hq, Dh = 32, 128
        Hkv = k_cache.shape[1]
        rep = Hq // Hkv
        q = (lambda x, *d: x.permute(*d))((x @ wq).reshape(B, Sq, Hq, Dh), 0, 2, 1, 3)
        k = (lambda x, n, axis: torch.repeat_interleave(x, n, dim=axis))(k_cache, rep, axis=1)
        v = (lambda x, n, axis: torch.repeat_interleave(x, n, dim=axis))(v_cache, rep, axis=1)
        scores = torch.einsum("bhqd,bhkd->bhqk", q, k) * (Dh ** -0.5)
        p = _softmax(scores, axis=-1)
        o = torch.einsum("bhqk,bhkd->bhqd", p, v)
        return (lambda x, *d: x.permute(*d))(o, 0, 2, 1, 3).reshape(B, Sq, Hq * Dh) @ wo

    def _B_ref(x, k_cache, v_cache, wq, wo):
        B, Sq, D = x.shape
        H, Dh = 32, 128
        q = (lambda x, *d: x.permute(*d))((x @ wq).reshape(B, Sq, H, Dh), 0, 2, 1, 3)
        scores = torch.einsum("bhqd,bhkd->bhqk", q, k_cache) * (Dh ** -0.5)
        p = _softmax(scores, axis=-1)
        o = torch.einsum("bhqk,bhkd->bhqd", p, v_cache)
        return (lambda x, *d: x.permute(*d))(o, 0, 2, 1, 3).reshape(B, Sq, H * Dh) @ wo

    a = _A_ref(A_x, A_k_cache, A_v_cache, A_wq, A_wo)
    b = _B_ref(B_x, B_k_cache, B_v_cache, B_wq, B_wo)
    return torch.concatenate([a, b], axis=-1)
```

```cpp
#include <hip/hip_runtime.h>
#include <hip/hip_bf16.h>
typedef __hip_bfloat16 bf16;
static __device__ __forceinline__ float b2f(bf16 v) { return __bfloat162float(v); }

__global__ void nq_kernel(const bf16* x, const bf16* wq, bf16* q) {
  int n = blockIdx.x * 256 + threadIdx.x; int row = blockIdx.y;
  float acc = 0.f;
  for (int k = 0; k < 512; ++k) acc += b2f(x[row * 512 + k]) * b2f(wq[(size_t)k * 4096 + n]);
  q[(size_t)row * 4096 + n] = __float2bfloat16(acc);
}
__global__ void nattn_kernel(const bf16* q, const bf16* kc, const bf16* vc, bf16* o, int hkv) {
  __shared__ float s[1024]; __shared__ float qs[128]; __shared__ float red[256];
  int qrow = blockIdx.x, h = blockIdx.y, b = blockIdx.z, t = threadIdx.x;
  int rep = 32 / hkv; int kvh = h / rep;
  const bf16* K = kc + ((size_t)(b * hkv + kvh)) * 1024 * 128;
  const bf16* V = vc + ((size_t)(b * hkv + kvh)) * 1024 * 128;
  if (t < 128) qs[t] = b2f(q[(size_t)(b * 80 + qrow) * 4096 + h * 128 + t]);
  __syncthreads();
  float mx = -1e30f;
  for (int k = t; k < 1024; k += 256) { float a = 0.f; for (int d = 0; d < 128; ++d) a += qs[d] * b2f(K[k * 128 + d]); a *= 0.08838834764831845f; s[k] = a; mx = fmaxf(mx, a); }
  red[t] = mx; __syncthreads();
  for (int o2 = 128; o2 > 0; o2 >>= 1) { if (t < o2) red[t] = fmaxf(red[t], red[t + o2]); __syncthreads(); }
  mx = red[0]; __syncthreads();
  float sum = 0.f;
  for (int k = t; k < 1024; k += 256) { float e = __expf(s[k] - mx); s[k] = e; sum += e; }
  red[t] = sum; __syncthreads();
  for (int o2 = 128; o2 > 0; o2 >>= 1) { if (t < o2) red[t] += red[t + o2]; __syncthreads(); }
  sum = red[0];
  if (t < 128) { float a = 0.f; for (int k = 0; k < 1024; ++k) a += s[k] * b2f(V[k * 128 + t]); o[(size_t)(b * 80 + qrow) * 4096 + h * 128 + t] = __float2bfloat16(a / sum); }
}
__global__ void no_kernel(const bf16* o, const bf16* wo, bf16* out, int coff) {
  int n = blockIdx.x * 256 + threadIdx.x; int row = blockIdx.y;
  float acc = 0.f;
  for (int k = 0; k < 4096; ++k) acc += b2f(o[(size_t)row * 4096 + k]) * b2f(wo[(size_t)k * 512 + n]);
  out[(size_t)row * 1024 + coff + n] = __float2bfloat16(acc);
}
extern "C" void kernel_launch(void* const* d_in, const int* in_sizes, int n_in, void* d_out, int out_size, void* d_ws, size_t ws_size, hipStream_t stream) {
  bf16* q = (bf16*)d_ws; bf16* o = q + 640 * 4096;
  for (int br = 0; br < 2; ++br) {
    const bf16* x = (const bf16*)d_in[5 * br + 0]; const bf16* kc = (const bf16*)d_in[5 * br + 1]; const bf16* vc = (const bf16*)d_in[5 * br + 2];
    const bf16* wq = (const bf16*)d_in[5 * br + 3]; const bf16* wo = (const bf16*)d_in[5 * br + 4];
    nq_kernel<<<dim3(16, 640), 256, 0, stream>>>(x, wq, q);
    nattn_kernel<<<dim3(80, 32, 8), 256, 0, stream>>>(q, kc, vc, o, br == 0 ? 8 : 32);
    no_kernel<<<dim3(2, 640), 256, 0, stream>>>(o, wo, (bf16*)d_out, br * 512);
  }
}
```

```cpp
#include <hip/hip_runtime.h>
#include <hip/hip_bf16.h>

typedef __attribute__((ext_vector_type(4))) unsigned u32x4;
#define DI __device__ __forceinline__
#pragma clang diagnostic ignored "-Winline-asm"

#define ATTN_ASM \
  "s_mov_b32 s63, %[isg]\n\t" \
  "s_cmp_lg_u32 s63, 0\n\t" \
  "s_cbranch_scc0 .Lqstart_%=\n\t" \
  "s_mov_b32 s92, %[tpre]\n\t" \
  "v_lshlrev_b32 v56, 3, %[l16]\n\t" \
  "v_min_u32 v57, 0x780, v56\n\t" \
  "global_load_dword v60, v56, %[xtb]\n\t" \
  "v_add_u32 v58, 0x2000, v57\n\t" \
  "global_load_dword v61, v58, %[xtb]\n\t" \
  "s_getpc_b64 s[56:57]\n\t" \
  ".Lhere0_%=:\n\t" \
  "v_add_u32 v59, %[cto], v57\n\t" \
  "v_min_u32 v59, .Lred_end-.Lhere0_%=-4, v59\n\t" \
  "global_load_dword v64, v59, s[56:57]\n\t" \
  "s_and_b32 s78, %[cto], 0x3fff\n\t" \
  "s_mul_i32 s78, s78, 5\n\t" \
  "buffer_load_dword v62, v56, %[srdX], s78 offen\n\t" \
  "buffer_load_dword v63, v58, %[srdX], s78 offen\n\t" \
  "v_xor_b32 v59, 122880, %[mrot]\n\t" \
  "global_load_dword v65, v59, %[mv]\n\t" \
  "v_xor_b32 v59, 114688, %[mrot]\n\t" \
  "global_load_dword v65, v59, %[mv]\n\t" \
  "v_xor_b32 v59, 122880, %[mrot]\n\t" \
  "global_load_dword v65, v59, %[mk]\n\t" \
  ".Lqstart_%=:\n\t" \
  "s_lshl_b32 s77, %[qrot], 7\n\t" \
  "s_mov_b32 s76, 43008\n\t" \
  "s_and_b32 s97, %[w8k], 0x2000\n\t" \
  "s_lshl_b32 s97, s97, 5\n\t" \
  "v_add_u32 v66, 31744, %[wo0]\n\t" \
  "v_xor_b32 v67, 0x80, %[wo0]\n\t" \
  "v_add_u32 v68, 95232, v67\n\t" \
  "v_add_u32 v67, 63488, v67\n\t" \
  "v_xor_b32 v69, 64, %[xo0]\n\t" \
  "v_add_u32 v69, 7168, v69\n\t" \
  "v_add_u32 v70, 14336, %[xo0]\n\t" \
  "s_mul_i32 s98, %[w8k], 3\n\t" \
  "s_lshr_b32 s98, s98, 3\n\t" \
  "s_min_u32 s98, s98, 7168\n\t" \
  "s_add_u32 s98, s98, 0x8000\n\t" \
  "v_mov_b32 v76, 0\n\t" \
  "v_mov_b32 v77, 0\n\t" \
  "v_mov_b32 v78, 0\n\t" \
  "v_mov_b32 v79, 0\n\t" \
  "s_add_u32 m0, s76, %[w8k]\n\t" \
  "s_lshl_b32 s71, s77, 12\n\t" \
  "s_add_u32 s71, s71, s97\n\t" \
  "buffer_load_dwordx4 %[wo0], %[srdW], s71 offen lds\n\t" \
  "buffer_load_dwordx4 v66, %[srdW], s71 offen offset:1024 lds\n\t" \
  "buffer_load_dwordx4 v67, %[srdW], s71 offen offset:2048 lds\n\t" \
  "buffer_load_dwordx4 v68, %[srdW], s71 offen offset:3072 lds\n\t" \
  "s_add_u32 m0, m0, 4096\n\t" \
  "s_add_u32 s71, s71, 0x20000\n\t" \
  "buffer_load_dwordx4 %[wo0], %[srdW], s71 offen lds\n\t" \
  "buffer_load_dwordx4 v66, %[srdW], s71 offen offset:1024 lds\n\t" \
  "buffer_load_dwordx4 v67, %[srdW], s71 offen offset:2048 lds\n\t" \
  "buffer_load_dwordx4 v68, %[srdW], s71 offen offset:3072 lds\n\t" \
  "s_add_u32 m0, s76, s98\n\t" \
  "s_add_u32 s76, s76, 43008\n\t" \
  "buffer_load_dwordx4 %[xo0], %[srdX], s77 offen lds\n\t" \
  "buffer_load_dwordx4 v69, %[srdX], s77 offen offset:1024 lds\n\t" \
  "buffer_load_dwordx4 v70, %[srdX], s77 offen offset:2048 lds\n\t" \
  "s_add_u32 s77, s77, 128\n\t" \
  "s_and_b32 s77, s77, 0x3ff\n\t" \
  "s_cmp_ge_u32 s76, 129024\n\t" \
  "s_cselect_b32 s76, 0, s76\n\t" \
  "v_mfma_f32_16x16x32_bf16 v[96:99], v[76:79], v[76:79], 0\n\t" \
  "s_lshr_b32 s96, %[w8k], 14\n\t" \
  "v_mfma_f32_16x16x32_bf16 v[100:103], v[76:79], v[76:79], 0\n\t" \
  "s_lshl_b32 s96, s96, 12\n\t" \
  "v_mfma_f32_16x16x32_bf16 v[104:107], v[76:79], v[76:79], 0\n\t" \
  "s_mov_b32 s67, 0\n\t" \
  "v_mfma_f32_16x16x32_bf16 v[108:111], v[76:79], v[76:79], 0\n\t" \
  "s_mov_b32 s68, 0\n\t" \
  "v_mfma_f32_16x16x32_bf16 v[112:115], v[76:79], v[76:79], 0\n\t" \
  "s_getpc_b64 s[56:57]\n\t" \
  ".Lhere_%=:\n\t" \
  "s_cmp_lg_u32 s63, 0\n\t" \
  "s_mov_b32 s78, .LfMK_%=-.Lhere_%=\n\t" \
  "s_cselect_b32 s78, .LfGK_%=-.Lhere_%=, s78\n\t" \
  "s_mov_b32 s72, .LfMV_%=-.Lhere_%=\n\t" \
  "s_cselect_b32 s72, .LfGV_%=-.Lhere_%=, s72\n\t" \
  "s_movk_i32 s62, 0x4000\n\t" \
  "s_cselect_b32 s62, s62, 0x8000\n\t" \
  "s_cselect_b32 s79, s96, 0\n\t" \
  "v_mfma_f32_16x16x32_bf16 v[116:119], v[76:79], v[76:79], 0\n\t" \
  "s_add_u32 s58, s56, s72\n\t" \
  "s_addc_u32 s59, s57, 0\n\t" \
  "v_mfma_f32_16x16x32_bf16 v[120:123], v[76:79], v[76:79], 0\n\t" \
  "s_add_u32 s56, s56, s78\n\t" \
  "s_addc_u32 s57, s57, 0\n\t" \
  "v_mfma_f32_16x16x32_bf16 v[124:127], v[76:79], v[76:79], 0\n\t" \
  "s_or_b32 s99, %[rot], s79\n\t" \
  "v_mfma_f32_16x16x32_bf16 v[128:131], v[76:79], v[76:79], 0\n\t" \
  "s_sub_u32 s65, s62, 1\n\t" \
  "s_and_b32 s65, s65, %[w8k]\n\t" \
  "s_add_u32 s65, s65, 0x20000\n\t" \
  "s_add_u32 s95, s65, s79\n\t" \
  "v_mfma_f32_16x16x32_bf16 v[132:135], v[76:79], v[76:79], 0\n\t" \
  "s_mov_b32 s73, 0\n\t" \
  "v_mfma_f32_16x16x32_bf16 v[136:139], v[76:79], v[76:79], 0\n\t" \
  "s_mov_b32 s74, 0x41000000\n\t" \
  "v_mfma_f32_16x16x32_bf16 v[140:143], v[76:79], v[76:79], 0\n\t" \
  "s_mov_b32 s80, 0x3e0293ee\n\t" \
  "v_mfma_f32_16x16x32_bf16 v[144:147], v[76:79], v[76:79], 0\n\t" \
  "s_mov_b32 s81, 0x3e0293ee\n\t" \
  "v_mfma_f32_16x16x32_bf16 v[148:151], v[76:79], v[76:79], 0\n\t" \
  "s_mov_b32 s75, 43008\n\t" \
  "v_mfma_f32_16x16x32_bf16 v[152:155], v[76:79], v[76:79], 0\n\t" \
  "s_mov_b32 s64, 0\n\t" \
  "v_mfma_f32_16x16x32_bf16 v[156:159], v[76:79], v[76:79], 0\n\t" \
  "v_xor_b32 v252, 64, %[ko0]\n\t" \
  "v_mfma_f32_16x16x32_bf16 v[160:163], v[76:79], v[76:79], 0\n\t" \
  "v_xor_b32 v253, 128, %[ko0]\n\t" \
  "v_mfma_f32_16x16x32_bf16 v[164:167], v[76:79], v[76:79], 0\n\t" \
  "v_xor_b32 v254, 192, %[ko0]\n\t" \
  "v_mfma_f32_16x16x32_bf16 v[168:171], v[76:79], v[76:79], 0\n\t" \
  "v_xor_b32 v255, 0x80, %[vo0]\n\t" \
  "v_mfma_f32_16x16x32_bf16 v[172:175], v[76:79], v[76:79], 0\n\t" \
  "v_mov_b32 v248, 0x3f803f80\n\t" \
  "v_mfma_f32_16x16x32_bf16 a[0:3], v[76:79], v[76:79], 0\n\t" \
  "v_accvgpr_write_b32 a240, v248\n\t" \
  "v_mfma_f32_16x16x32_bf16 a[4:7], v[76:79], v[76:79], 0\n\t" \
  "v_accvgpr_write_b32 a241, v248\n\t" \
  "v_mfma_f32_16x16x32_bf16 a[8:11], v[76:79], v[76:79], 0\n\t" \
  "v_accvgpr_write_b32 a242, v248\n\t" \
  "v_mfma_f32_16x16x32_bf16 a[12:15], v[76:79], v[76:79], 0\n\t" \
  "v_accvgpr_write_b32 a243, v248\n\t" \
  "v_mfma_f32_16x16x32_bf16 a[16:19], v[76:79], v[76:79], 0\n\t" \
  "v_add_u32 v248, s75, %[wr]\n\t" \
  "v_mfma_f32_16x16x32_bf16 a[20:23], v[76:79], v[76:79], 0\n\t" \
  "v_add_u32 v71, s75, %[xr]\n\t" \
  "v_mfma_f32_16x16x32_bf16 a[24:27], v[76:79], v[76:79], 0\n\t" \
  "v_xor_b32 v249, 32, v248\n\t" \
  "v_mfma_f32_16x16x32_bf16 a[28:31], v[76:79], v[76:79], 0\n\t" \
  "v_xor_b32 v250, 64, v248\n\t" \
  "v_mfma_f32_16x16x32_bf16 a[32:35], v[76:79], v[76:79], 0\n\t" \
  "v_xor_b32 v251, 96, v248\n\t" \
  "v_mfma_f32_16x16x32_bf16 a[36:39], v[76:79], v[76:79], 0\n\t" \
  "v_xor_b32 v72, 64, v71\n\t" \
  "v_mfma_f32_16x16x32_bf16 a[40:43], v[76:79], v[76:79], 0\n\t" \
  "v_mfma_f32_16x16x32_bf16 a[44:47], v[76:79], v[76:79], 0\n\t" \
  "v_mfma_f32_16x16x32_bf16 a[48:51], v[76:79], v[76:79], 0\n\t" \
  "s_add_u32 m0, s76, %[w8k]\n\t" \
  "s_lshl_b32 s71, s77, 12\n\t" \
  "s_add_u32 s71, s71, s97\n\t" \
  "buffer_load_dwordx4 %[wo0], %[srdW], s71 offen lds\n\t" \
  "v_mfma_f32_16x16x32_bf16 a[52:55], v[76:79], v[76:79], 0\n\t" \
  "buffer_load_dwordx4 v66, %[srdW], s71 offen offset:1024 lds\n\t" \
  "v_mfma_f32_16x16x32_bf16 a[56:59], v[76:79], v[76:79], 0\n\t" \
  "v_mfma_f32_16x16x32_bf16 a[60:63], v[76:79], v[76:79], 0\n\t" \
  "buffer_load_dwordx4 v67, %[srdW], s71 offen offset:2048 lds\n\t" \
  "v_mfma_f32_16x16x32_bf16 a[64:67], v[76:79], v[76:79], 0\n\t" \
  "v_mfma_f32_16x16x32_bf16 a[68:71], v[76:79], v[76:79], 0\n\t" \
  "buffer_load_dwordx4 v68, %[srdW], s71 offen offset:3072 lds\n\t" \
  "v_mfma_f32_16x16x32_bf16 a[72:75], v[76:79], v[76:79], 0\n\t" \
  "v_mfma_f32_16x16x32_bf16 a[76:79], v[76:79], v[76:79], 0\n\t" \
  "s_add_u32 m0, m0, 4096\n\t" \
  "s_add_u32 s71, s71, 0x20000\n\t" \
  "buffer_load_dwordx4 %[wo0], %[srdW], s71 offen lds\n\t" \
  "v_mfma_f32_16x16x32_bf16 a[80:83], v[76:79], v[76:79], 0\n\t" \
  "v_mfma_f32_16x16x32_bf16 a[84:87], v[76:79], v[76:79], 0\n\t" \
  "buffer_load_dwordx4 v66, %[srdW], s71 offen offset:1024 lds\n\t" \
  "v_mfma_f32_16x16x32_bf16 a[88:91], v[76:79], v[76:79], 0\n\t" \
  "v_mfma_f32_16x16x32_bf16 a[92:95], v[76:79], v[76:79], 0\n\t" \
  "buffer_load_dwordx4 v67, %[srdW], s71 offen offset:2048 lds\n\t" \
  "v_mfma_f32_16x16x32_bf16 a[96:99], v[76:79], v[76:79], 0\n\t" \
  "v_mfma_f32_16x16x32_bf16 a[100:103], v[76:79], v[76:79], 0\n\t" \
  "buffer_load_dwordx4 v68, %[srdW], s71 offen offset:3072 lds\n\t" \
  "v_mfma_f32_16x16x32_bf16 a[104:107], v[76:79], v[76:79], 0\n\t" \
  "v_mfma_f32_16x16x32_bf16 a[108:111], v[76:79], v[76:79], 0\n\t" \
  "s_add_u32 m0, s76, s98\n\t" \
  "s_add_u32 s76, s76, 43008\n\t" \
  "buffer_load_dwordx4 %[xo0], %[srdX], s77 offen lds\n\t" \
  "v_mfma_f32_16x16x32_bf16 a[112:115], v[76:79], v[76:79], 0\n\t" \
  "buffer_load_dwordx4 v69, %[srdX], s77 offen offset:1024 lds\n\t" \
  "v_mfma_f32_16x16x32_bf16 a[116:119], v[76:79], v[76:79], 0\n\t" \
  "v_mfma_f32_16x16x32_bf16 a[120:123], v[76:79], v[76:79], 0\n\t" \
  "buffer_load_dwordx4 v70, %[srdX], s77 offen offset:2048 lds\n\t" \
  "s_add_u32 s77, s77, 128\n\t" \
  "s_and_b32 s77, s77, 0x3ff\n\t" \
  "s_cmp_ge_u32 s76, 129024\n\t" \
  "s_cselect_b32 s76, 0, s76\n\t" \
  "v_mfma_f32_16x16x32_bf16 a[124:127], v[76:79], v[76:79], 0\n\t" \
  "v_mfma_f32_16x16x32_bf16 a[128:131], v[76:79], v[76:79], 0\n\t" \
  "s_add_u32 m0, s76, %[w8k]\n\t" \
  "s_lshl_b32 s71, s77, 12\n\t" \
  "s_add_u32 s71, s71, s97\n\t" \
  "buffer_load_dwordx4 %[wo0], %[srdW], s71 offen lds\n\t" \
  "v_mfma_f32_16x16x32_bf16 a[132:135], v[76:79], v[76:79], 0\n\t" \
  "v_mfma_f32_16x16x32_bf16 a[136:139], v[76:79], v[76:79], 0\n\t" \
  "buffer_load_dwordx4 v66, %[srdW], s71 offen offset:1024 lds\n\t" \
  "v_mfma_f32_16x16x32_bf16 a[140:143], v[76:79], v[76:79], 0\n\t" \
  "v_mfma_f32_16x16x32_bf16 a[144:147], v[76:79], v[76:79], 0\n\t" \
  "buffer_load_dwordx4 v67, %[srdW], s71 offen offset:2048 lds\n\t" \
  "v_mfma_f32_16x16x32_bf16 a[148:151], v[76:79], v[76:79], 0\n\t" \
  "v_mfma_f32_16x16x32_bf16 a[152:155], v[76:79], v[76:79], 0\n\t" \
  "buffer_load_dwordx4 v68, %[srdW], s71 offen offset:3072 lds\n\t" \
  "v_mfma_f32_16x16x32_bf16 a[156:159], v[76:79], v[76:79], 0\n\t" \
  "v_mfma_f32_16x16x32_bf16 a[160:163], v[76:79], v[76:79], 0\n\t" \
  "s_add_u32 m0, m0, 4096\n\t" \
  "s_add_u32 s71, s71, 0x20000\n\t" \
  "buffer_load_dwordx4 %[wo0], %[srdW], s71 offen lds\n\t" \
  "v_mfma_f32_16x16x32_bf16 a[164:167], v[76:79], v[76:79], 0\n\t" \
  "v_mfma_f32_16x16x32_bf16 a[168:171], v[76:79], v[76:79], 0\n\t" \
  "buffer_load_dwordx4 v66, %[srdW], s71 offen offset:1024 lds\n\t" \
  "v_mfma_f32_16x16x32_bf16 a[172:175], v[76:79], v[76:79], 0\n\t" \
  "v_mfma_f32_16x16x32_bf16 a[176:179], v[76:79], v[76:79], 0\n\t" \
  "s_waitcnt vmcnt(17)\n\t" \
  "s_barrier\n\t" \
  "ds_read_b128 v[208:211], v71 offset:0\n\t" \
  "ds_read_b128 v[212:215], v71 offset:2048\n\t" \
  "ds_read_b128 v[216:219], v71 offset:4096\n\t" \
  "ds_read_b128 v[220:223], v71 offset:6144\n\t" \
  "ds_read_b128 v[224:227], v71 offset:8192\n\t" \
  "ds_read_b64_tr_b16 v[176:177], v248 offset:0\n\t" \
  "ds_read_b64_tr_b16 v[178:179], v248 offset:1024\n\t" \
  "ds_read_b64_tr_b16 v[180:181], v249 offset:0\n\t" \
  "ds_read_b64_tr_b16 v[182:183], v249 offset:1024\n\t" \
  "ds_read_b64_tr_b16 v[184:185], v250 offset:0\n\t" \
  "ds_read_b64_tr_b16 v[186:187], v250 offset:1024\n\t" \
  "ds_read_b64_tr_b16 v[188:189], v251 offset:0\n\t" \
  "ds_read_b64_tr_b16 v[190:191], v251 offset:1024\n\t" \
  ".Lqloop_%=:\n\t" \
  "s_waitcnt lgkmcnt(0)\n\t" \
  "v_mfma_f32_16x16x32_bf16 v[96:99], v[176:179], v[208:211], v[96:99]\n\t" \
  "ds_read_b128 v[228:231], v72 offset:0\n\t" \
  "v_mfma_f32_16x16x32_bf16 v[100:103], v[176:179], v[212:215], v[100:103]\n\t" \
  "ds_read_b128 v[232:235], v72 offset:2048\n\t" \
  "buffer_load_dwordx4 v67, %[srdW], s71 offen offset:2048 lds\n\t" \
  "v_mfma_f32_16x16x32_bf16 v[104:107], v[176:179], v[216:219], v[104:107]\n\t" \
  "ds_read_b128 v[236:239], v72 offset:4096\n\t" \
  "v_mfma_f32_16x16x32_bf16 v[108:111], v[176:179], v[220:223], v[108:111]\n\t" \
  "ds_read_b128 v[240:243], v72 offset:6144\n\t" \
  "v_mfma_f32_16x16x32_bf16 v[112:115], v[176:179], v[224:227], v[112:115]\n\t" \
  "ds_read_b128 v[244:247], v72 offset:8192\n\t" \
  "v_mfma_f32_16x16x32_bf16 v[116:119], v[180:183], v[208:211], v[116:119]\n\t" \
  "ds_read_b64_tr_b16 v[192:193], v248 offset:8192\n\t" \
  "buffer_load_dwordx4 v68, %[srdW], s71 offen offset:3072 lds\n\t" \
  "v_mfma_f32_16x16x32_bf16 v[120:123], v[180:183], v[212:215], v[120:123]\n\t" \
  "ds_read_b64_tr_b16 v[194:195], v248 offset:9216\n\t" \
  "v_mfma_f32_16x16x32_bf16 v[124:127], v[180:183], v[216:219], v[124:127]\n\t" \
  "ds_read_b64_tr_b16 v[196:197], v249 offset:8192\n\t" \
  "v_mfma_f32_16x16x32_bf16 v[128:131], v[180:183], v[220:223], v[128:131]\n\t" \
  "ds_read_b64_tr_b16 v[198:199], v249 offset:9216\n\t" \
  "v_mfma_f32_16x16x32_bf16 v[132:135], v[180:183], v[224:227], v[132:135]\n\t" \
  "ds_read_b64_tr_b16 v[200:201], v250 offset:8192\n\t" \
  "s_add_u32 m0, s76, s98\n\t" \
  "s_add_u32 s76, s76, 43008\n\t" \
  "buffer_load_dwordx4 %[xo0], %[srdX], s77 offen lds\n\t" \
  "v_mfma_f32_16x16x32_bf16 v[136:139], v[184:187], v[208:211], v[136:139]\n\t" \
  "ds_read_b64_tr_b16 v[202:203], v250 offset:9216\n\t" \
  "v_mfma_f32_16x16x32_bf16 v[140:143], v[184:187], v[212:215], v[140:143]\n\t" \
  "ds_read_b64_tr_b16 v[204:205], v251 offset:8192\n\t" \
  "v_mfma_f32_16x16x32_bf16 v[144:147], v[184:187], v[216:219], v[144:147]\n\t" \
  "ds_read_b64_tr_b16 v[206:207], v251 offset:9216\n\t" \
  "v_mfma_f32_16x16x32_bf16 v[148:151], v[184:187], v[220:223], v[148:151]\n\t" \
  "buffer_load_dwordx4 v69, %[srdX], s77 offen offset:1024 lds\n\t" \
  "s_add_u32 s75, s75, 43008\n\t" \
  "s_cmp_ge_u32 s75, 129024\n\t" \
  "s_cselect_b32 s75, 0, s75\n\t" \
  "v_mfma_f32_16x16x32_bf16 v[152:155], v[184:187], v[224:227], v[152:155]\n\t" \
  "v_add_u32 v248, s75, %[wr]\n\t" \
  "v_mfma_f32_16x16x32_bf16 v[156:159], v[188:191], v[208:211], v[156:159]\n\t" \
  "v_add_u32 v71, s75, %[xr]\n\t" \
  "v_mfma_f32_16x16x32_bf16 v[160:163], v[188:191], v[212:215], v[160:163]\n\t" \
  "v_xor_b32 v249, 32, v248\n\t" \
  "v_mfma_f32_16x16x32_bf16 v[164:167], v[188:191], v[216:219], v[164:167]\n\t" \
  "buffer_load_dwordx4 v70, %[srdX], s77 offen offset:2048 lds\n\t" \
  "s_add_u32 s77, s77, 128\n\t" \
  "s_and_b32 s77, s77, 0x3ff\n\t" \
  "s_cmp_ge_u32 s76, 129024\n\t" \
  "s_cselect_b32 s76, 0, s76\n\t" \
  "v_xor_b32 v250, 64, v248\n\t" \
  "v_mfma_f32_16x16x32_bf16 v[168:171], v[188:191], v[220:223], v[168:171]\n\t" \
  "v_xor_b32 v251, 96, v248\n\t" \
  "v_mfma_f32_16x16x32_bf16 v[172:175], v[188:191], v[224:227], v[172:175]\n\t" \
  "v_xor_b32 v72, 64, v71\n\t" \
  "s_waitcnt vmcnt(11) lgkmcnt(0)\n\t" \
  "s_barrier\n\t" \
  "v_mfma_f32_16x16x32_bf16 v[96:99], v[192:195], v[228:231], v[96:99]\n\t" \
  "ds_read_b128 v[208:211], v71 offset:0\n\t" \
  "v_mfma_f32_16x16x32_bf16 v[100:103], v[192:195], v[232:235], v[100:103]\n\t" \
  "ds_read_b128 v[212:215], v71 offset:2048\n\t" \
  "s_add_u32 m0, s76, %[w8k]\n\t" \
  "s_lshl_b32 s71, s77, 12\n\t" \
  "s_add_u32 s71, s71, s97\n\t" \
  "buffer_load_dwordx4 %[wo0], %[srdW], s71 offen lds\n\t" \
  "v_mfma_f32_16x16x32_bf16 v[104:107], v[192:195], v[236:239], v[104:107]\n\t" \
  "ds_read_b128 v[216:219], v71 offset:4096\n\t" \
  "v_mfma_f32_16x16x32_bf16 v[108:111], v[192:195], v[240:243], v[108:111]\n\t" \
  "ds_read_b128 v[220:223], v71 offset:6144\n\t" \
  "v_mfma_f32_16x16x32_bf16 v[112:115], v[192:195], v[244:247], v[112:115]\n\t" \
  "ds_read_b128 v[224:227], v71 offset:8192\n\t" \
  "buffer_load_dwordx4 v66, %[srdW], s71 offen offset:1024 lds\n\t" \
  "v_mfma_f32_16x16x32_bf16 v[116:119], v[196:199], v[228:231], v[116:119]\n\t" \
  "ds_read_b64_tr_b16 v[176:177], v248 offset:0\n\t" \
  "v_mfma_f32_16x16x32_bf16 v[120:123], v[196:199], v[232:235], v[120:123]\n\t" \
  "ds_read_b64_tr_b16 v[178:179], v248 offset:1024\n\t" \
  "v_mfma_f32_16x16x32_bf16 v[124:127], v[196:199], v[236:239], v[124:127]\n\t" \
  "ds_read_b64_tr_b16 v[180:181], v249 offset:0\n\t" \
  "buffer_load_dwordx4 v67, %[srdW], s71 offen offset:2048 lds\n\t" \
  "v_mfma_f32_16x16x32_bf16 v[128:131], v[196:199], v[240:243], v[128:131]\n\t" \
  "ds_read_b64_tr_b16 v[182:183], v249 offset:1024\n\t" \
  "v_mfma_f32_16x16x32_bf16 v[132:135], v[196:199], v[244:247], v[132:135]\n\t" \
  "ds_read_b64_tr_b16 v[184:185], v250 offset:0\n\t" \
  "v_mfma_f32_16x16x32_bf16 v[136:139], v[200:203], v[228:231], v[136:139]\n\t" \
  "ds_read_b64_tr_b16 v[186:187], v250 offset:1024\n\t" \
  "buffer_load_dwordx4 v68, %[srdW], s71 offen offset:3072 lds\n\t" \
  "v_mfma_f32_16x16x32_bf16 v[140:143], v[200:203], v[232:235], v[140:143]\n\t" \
  "ds_read_b64_tr_b16 v[188:189], v251 offset:0\n\t" \
  "v_mfma_f32_16x16x32_bf16 v[144:147], v[200:203], v[236:239], v[144:147]\n\t" \
  "ds_read_b64_tr_b16 v[190:191], v251 offset:1024\n\t" \
  "v_mfma_f32_16x16x32_bf16 v[148:151], v[200:203], v[240:243], v[148:151]\n\t" \
  "s_add_u32 m0, m0, 4096\n\t" \
  "s_add_u32 s71, s71, 0x20000\n\t" \
  "buffer_load_dwordx4 %[wo0], %[srdW], s71 offen lds\n\t" \
  "v_mfma_f32_16x16x32_bf16 v[152:155], v[200:203], v[244:247], v[152:155]\n\t" \
  "v_mfma_f32_16x16x32_bf16 v[156:159], v[204:207], v[228:231], v[156:159]\n\t" \
  "v_mfma_f32_16x16x32_bf16 v[160:163], v[204:207], v[232:235], v[160:163]\n\t" \
  "buffer_load_dwordx4 v66, %[srdW], s71 offen offset:1024 lds\n\t" \
  "v_mfma_f32_16x16x32_bf16 v[164:167], v[204:207], v[236:239], v[164:167]\n\t" \
  "v_mfma_f32_16x16x32_bf16 v[168:171], v[204:207], v[240:243], v[168:171]\n\t" \
  "s_add_u32 s64, s64, 1\n\t" \
  "s_cmp_lt_u32 s64, 5\n\t" \
  "v_mfma_f32_16x16x32_bf16 v[172:175], v[204:207], v[244:247], v[172:175]\n\t" \
  "s_cbranch_scc1 .Lqloop_%=\n\t" \
  "s_waitcnt lgkmcnt(0)\n\t" \
  "v_mfma_f32_16x16x32_bf16 v[96:99], v[176:179], v[208:211], v[96:99]\n\t" \
  "ds_read_b128 v[228:231], v72 offset:0\n\t" \
  "v_mfma_f32_16x16x32_bf16 v[100:103], v[176:179], v[212:215], v[100:103]\n\t" \
  "ds_read_b128 v[232:235], v72 offset:2048\n\t" \
  "buffer_load_dwordx4 v67, %[srdW], s71 offen offset:2048 lds\n\t" \
  "v_mfma_f32_16x16x32_bf16 v[104:107], v[176:179], v[216:219], v[104:107]\n\t" \
  "ds_read_b128 v[236:239], v72 offset:4096\n\t" \
  "v_mfma_f32_16x16x32_bf16 v[108:111], v[176:179], v[220:223], v[108:111]\n\t" \
  "ds_read_b128 v[240:243], v72 offset:6144\n\t" \
  "v_mfma_f32_16x16x32_bf16 v[112:115], v[176:179], v[224:227], v[112:115]\n\t" \
  "ds_read_b128 v[244:247], v72 offset:8192\n\t" \
  "v_mfma_f32_16x16x32_bf16 v[116:119], v[180:183], v[208:211], v[116:119]\n\t" \
  "ds_read_b64_tr_b16 v[192:193], v248 offset:8192\n\t" \
  "buffer_load_dwordx4 v68, %[srdW], s71 offen offset:3072 lds\n\t" \
  "v_mfma_f32_16x16x32_bf16 v[120:123], v[180:183], v[212:215], v[120:123]\n\t" \
  "ds_read_b64_tr_b16 v[194:195], v248 offset:9216\n\t" \
  "v_mfma_f32_16x16x32_bf16 v[124:127], v[180:183], v[216:219], v[124:127]\n\t" \
  "ds_read_b64_tr_b16 v[196:197], v249 offset:8192\n\t" \
  "v_mfma_f32_16x16x32_bf16 v[128:131], v[180:183], v[220:223], v[128:131]\n\t" \
  "ds_read_b64_tr_b16 v[198:199], v249 offset:9216\n\t" \
  "v_mfma_f32_16x16x32_bf16 v[132:135], v[180:183], v[224:227], v[132:135]\n\t" \
  "ds_read_b64_tr_b16 v[200:201], v250 offset:8192\n\t" \
  "s_add_u32 m0, s76, s98\n\t" \
  "s_add_u32 s76, s76, 43008\n\t" \
  "buffer_load_dwordx4 %[xo0], %[srdX], s77 offen lds\n\t" \
  "v_mfma_f32_16x16x32_bf16 v[136:139], v[184:187], v[208:211], v[136:139]\n\t" \
  "ds_read_b64_tr_b16 v[202:203], v250 offset:9216\n\t" \
  "v_mfma_f32_16x16x32_bf16 v[140:143], v[184:187], v[212:215], v[140:143]\n\t" \
  "ds_read_b64_tr_b16 v[204:205], v251 offset:8192\n\t" \
  "v_mfma_f32_16x16x32_bf16 v[144:147], v[184:187], v[216:219], v[144:147]\n\t" \
  "ds_read_b64_tr_b16 v[206:207], v251 offset:9216\n\t" \
  "v_mfma_f32_16x16x32_bf16 v[148:151], v[184:187], v[220:223], v[148:151]\n\t" \
  "buffer_load_dwordx4 v69, %[srdX], s77 offen offset:1024 lds\n\t" \
  "s_add_u32 s75, s75, 43008\n\t" \
  "s_cmp_ge_u32 s75, 129024\n\t" \
  "s_cselect_b32 s75, 0, s75\n\t" \
  "v_mfma_f32_16x16x32_bf16 v[152:155], v[184:187], v[224:227], v[152:155]\n\t" \
  "v_add_u32 v248, s75, %[wr]\n\t" \
  "v_mfma_f32_16x16x32_bf16 v[156:159], v[188:191], v[208:211], v[156:159]\n\t" \
  "v_add_u32 v71, s75, %[xr]\n\t" \
  "v_mfma_f32_16x16x32_bf16 v[160:163], v[188:191], v[212:215], v[160:163]\n\t" \
  "v_xor_b32 v249, 32, v248\n\t" \
  "v_mfma_f32_16x16x32_bf16 v[164:167], v[188:191], v[216:219], v[164:167]\n\t" \
  "buffer_load_dwordx4 v70, %[srdX], s77 offen offset:2048 lds\n\t" \
  "s_add_u32 s77, s77, 128\n\t" \
  "s_and_b32 s77, s77, 0x3ff\n\t" \
  "s_cmp_ge_u32 s76, 129024\n\t" \
  "s_cselect_b32 s76, 0, s76\n\t" \
  "v_xor_b32 v250, 64, v248\n\t" \
  "v_mfma_f32_16x16x32_bf16 v[168:171], v[188:191], v[220:223], v[168:171]\n\t" \
  "v_xor_b32 v251, 96, v248\n\t" \
  "v_mfma_f32_16x16x32_bf16 v[172:175], v[188:191], v[224:227], v[172:175]\n\t" \
  "v_xor_b32 v72, 64, v71\n\t" \
  "s_waitcnt vmcnt(11) lgkmcnt(0)\n\t" \
  "s_barrier\n\t" \
  "v_mfma_f32_16x16x32_bf16 v[96:99], v[192:195], v[228:231], v[96:99]\n\t" \
  "ds_read_b128 v[208:211], v71 offset:0\n\t" \
  "v_mfma_f32_16x16x32_bf16 v[100:103], v[192:195], v[232:235], v[100:103]\n\t" \
  "ds_read_b128 v[212:215], v71 offset:2048\n\t" \
  "v_mfma_f32_16x16x32_bf16 v[104:107], v[192:195], v[236:239], v[104:107]\n\t" \
  "ds_read_b128 v[216:219], v71 offset:4096\n\t" \
  "v_mfma_f32_16x16x32_bf16 v[108:111], v[192:195], v[240:243], v[108:111]\n\t" \
  "ds_read_b128 v[220:223], v71 offset:6144\n\t" \
  "v_mfma_f32_16x16x32_bf16 v[112:115], v[192:195], v[244:247], v[112:115]\n\t" \
  "ds_read_b128 v[224:227], v71 offset:8192\n\t" \
  "v_mfma_f32_16x16x32_bf16 v[116:119], v[196:199], v[228:231], v[116:119]\n\t" \
  "ds_read_b64_tr_b16 v[176:177], v248 offset:0\n\t" \
  "v_mfma_f32_16x16x32_bf16 v[120:123], v[196:199], v[232:235], v[120:123]\n\t" \
  "ds_read_b64_tr_b16 v[178:179], v248 offset:1024\n\t" \
  "v_mfma_f32_16x16x32_bf16 v[124:127], v[196:199], v[236:239], v[124:127]\n\t" \
  "ds_read_b64_tr_b16 v[180:181], v249 offset:0\n\t" \
  "v_mfma_f32_16x16x32_bf16 v[128:131], v[196:199], v[240:243], v[128:131]\n\t" \
  "ds_read_b64_tr_b16 v[182:183], v249 offset:1024\n\t" \
  "v_mfma_f32_16x16x32_bf16 v[132:135], v[196:199], v[244:247], v[132:135]\n\t" \
  "ds_read_b64_tr_b16 v[184:185], v250 offset:0\n\t" \
  "v_mfma_f32_16x16x32_bf16 v[136:139], v[200:203], v[228:231], v[136:139]\n\t" \
  "ds_read_b64_tr_b16 v[186:187], v250 offset:1024\n\t" \
  "v_mfma_f32_16x16x32_bf16 v[140:143], v[200:203], v[232:235], v[140:143]\n\t" \
  "ds_read_b64_tr_b16 v[188:189], v251 offset:0\n\t" \
  "v_mfma_f32_16x16x32_bf16 v[144:147], v[200:203], v[236:239], v[144:147]\n\t" \
  "ds_read_b64_tr_b16 v[190:191], v251 offset:1024\n\t" \
  "v_mfma_f32_16x16x32_bf16 v[148:151], v[200:203], v[240:243], v[148:151]\n\t" \
  "s_swappc_b64 s[60:61], s[56:57]\n\t" \
  "s_cmp_lg_u32 s63, 0\n\t" \
  "s_cbranch_scc0 .Lce0_%=\n\t" \
  "s_swappc_b64 s[60:61], s[56:57]\n\t" \
  ".Lce0_%=:\n\t" \
  "v_mfma_f32_16x16x32_bf16 v[152:155], v[200:203], v[244:247], v[152:155]\n\t" \
  "v_mfma_f32_16x16x32_bf16 v[156:159], v[204:207], v[228:231], v[156:159]\n\t" \
  "v_mfma_f32_16x16x32_bf16 v[160:163], v[204:207], v[232:235], v[160:163]\n\t" \
  "v_mfma_f32_16x16x32_bf16 v[164:167], v[204:207], v[236:239], v[164:167]\n\t" \
  "v_mfma_f32_16x16x32_bf16 v[168:171], v[204:207], v[240:243], v[168:171]\n\t" \
  "v_mfma_f32_16x16x32_bf16 v[172:175], v[204:207], v[244:247], v[172:175]\n\t" \
  "s_waitcnt lgkmcnt(0)\n\t" \
  "v_mfma_f32_16x16x32_bf16 v[96:99], v[176:179], v[208:211], v[96:99]\n\t" \
  "ds_read_b128 v[228:231], v72 offset:0\n\t" \
  "v_mfma_f32_16x16x32_bf16 v[100:103], v[176:179], v[212:215], v[100:103]\n\t" \
  "ds_read_b128 v[232:235], v72 offset:2048\n\t" \
  "v_mfma_f32_16x16x32_bf16 v[104:107], v[176:179], v[216:219], v[104:107]\n\t" \
  "ds_read_b128 v[236:239], v72 offset:4096\n\t" \
  "v_mfma_f32_16x16x32_bf16 v[108:111], v[176:179], v[220:223], v[108:111]\n\t" \
  "ds_read_b128 v[240:243], v72 offset:6144\n\t" \
  "v_mfma_f32_16x16x32_bf16 v[112:115], v[176:179], v[224:227], v[112:115]\n\t" \
  "ds_read_b128 v[244:247], v72 offset:8192\n\t" \
  "v_mfma_f32_16x16x32_bf16 v[116:119], v[180:183], v[208:211], v[116:119]\n\t" \
  "ds_read_b64_tr_b16 v[192:193], v248 offset:8192\n\t" \
  "v_mfma_f32_16x16x32_bf16 v[120:123], v[180:183], v[212:215], v[120:123]\n\t" \
  "ds_read_b64_tr_b16 v[194:195], v248 offset:9216\n\t" \
  "v_mfma_f32_16x16x32_bf16 v[124:127], v[180:183], v[216:219], v[124:127]\n\t" \
  "ds_read_b64_tr_b16 v[196:197], v249 offset:8192\n\t" \
  "v_mfma_f32_16x16x32_bf16 v[128:131], v[180:183], v[220:223], v[128:131]\n\t" \
  "ds_read_b64_tr_b16 v[198:199], v249 offset:9216\n\t" \
  "v_mfma_f32_16x16x32_bf16 v[132:135], v[180:183], v[224:227], v[132:135]\n\t" \
  "ds_read_b64_tr_b16 v[200:201], v250 offset:8192\n\t" \
  "v_mfma_f32_16x16x32_bf16 v[136:139], v[184:187], v[208:211], v[136:139]\n\t" \
  "ds_read_b64_tr_b16 v[202:203], v250 offset:9216\n\t" \
  "v_mfma_f32_16x16x32_bf16 v[140:143], v[184:187], v[212:215], v[140:143]\n\t" \
  "ds_read_b64_tr_b16 v[204:205], v251 offset:8192\n\t" \
  "v_mfma_f32_16x16x32_bf16 v[144:147], v[184:187], v[216:219], v[144:147]\n\t" \
  "ds_read_b64_tr_b16 v[206:207], v251 offset:9216\n\t" \
  "v_mfma_f32_16x16x32_bf16 v[148:151], v[184:187], v[220:223], v[148:151]\n\t" \
  "s_add_u32 s75, s75, 43008\n\t" \
  "s_cmp_ge_u32 s75, 129024\n\t" \
  "s_cselect_b32 s75, 0, s75\n\t" \
  "v_mfma_f32_16x16x32_bf16 v[152:155], v[184:187], v[224:227], v[152:155]\n\t" \
  "v_add_u32 v248, s75, %[wr]\n\t" \
  "v_mfma_f32_16x16x32_bf16 v[156:159], v[188:191], v[208:211], v[156:159]\n\t" \
  "v_add_u32 v71, s75, %[xr]\n\t" \
  "v_mfma_f32_16x16x32_bf16 v[160:163], v[188:191], v[212:215], v[160:163]\n\t" \
  "v_xor_b32 v249, 32, v248\n\t" \
  "v_mfma_f32_16x16x32_bf16 v[164:167], v[188:191], v[216:219], v[164:167]\n\t" \
  "v_xor_b32 v250, 64, v248\n\t" \
  "v_mfma_f32_16x16x32_bf16 v[168:171], v[188:191], v[220:223], v[168:171]\n\t" \
  "v_xor_b32 v251, 96, v248\n\t" \
  "v_mfma_f32_16x16x32_bf16 v[172:175], v[188:191], v[224:227], v[172:175]\n\t" \
  "v_xor_b32 v72, 64, v71\n\t" \
  "s_waitcnt vmcnt(8) lgkmcnt(0)\n\t" \
  "s_barrier\n\t" \
  "v_mfma_f32_16x16x32_bf16 v[96:99], v[192:195], v[228:231], v[96:99]\n\t" \
  "ds_read_b128 v[208:211], v71 offset:0\n\t" \
  "v_mfma_f32_16x16x32_bf16 v[100:103], v[192:195], v[232:235], v[100:103]\n\t" \
  "ds_read_b128 v[212:215], v71 offset:2048\n\t" \
  "v_mfma_f32_16x16x32_bf16 v[104:107], v[192:195], v[236:239], v[104:107]\n\t" \
  "ds_read_b128 v[216:219], v71 offset:4096\n\t" \
  "v_mfma_f32_16x16x32_bf16 v[108:111], v[192:195], v[240:243], v[108:111]\n\t" \
  "ds_read_b128 v[220:223], v71 offset:6144\n\t" \
  "v_mfma_f32_16x16x32_bf16 v[112:115], v[192:195], v[244:247], v[112:115]\n\t" \
  "ds_read_b128 v[224:227], v71 offset:8192\n\t" \
  "v_mfma_f32_16x16x32_bf16 v[116:119], v[196:199], v[228:231], v[116:119]\n\t" \
  "ds_read_b64_tr_b16 v[176:177], v248 offset:0\n\t" \
  "v_mfma_f32_16x16x32_bf16 v[120:123], v[196:199], v[232:235], v[120:123]\n\t" \
  "ds_read_b64_tr_b16 v[178:179], v248 offset:1024\n\t" \
  "v_mfma_f32_16x16x32_bf16 v[124:127], v[196:199], v[236:239], v[124:127]\n\t" \
  "ds_read_b64_tr_b16 v[180:181], v249 offset:0\n\t" \
  "v_mfma_f32_16x16x32_bf16 v[128:131], v[196:199], v[240:243], v[128:131]\n\t" \
  "ds_read_b64_tr_b16 v[182:183], v249 offset:1024\n\t" \
  "v_mfma_f32_16x16x32_bf16 v[132:135], v[196:199], v[244:247], v[132:135]\n\t" \
  "ds_read_b64_tr_b16 v[184:185], v250 offset:0\n\t" \
  "v_mfma_f32_16x16x32_bf16 v[136:139], v[200:203], v[228:231], v[136:139]\n\t" \
  "ds_read_b64_tr_b16 v[186:187], v250 offset:1024\n\t" \
  "v_mfma_f32_16x16x32_bf16 v[140:143], v[200:203], v[232:235], v[140:143]\n\t" \
  "ds_read_b64_tr_b16 v[188:189], v251 offset:0\n\t" \
  "v_mfma_f32_16x16x32_bf16 v[144:147], v[200:203], v[236:239], v[144:147]\n\t" \
  "ds_read_b64_tr_b16 v[190:191], v251 offset:1024\n\t" \
  "v_mfma_f32_16x16x32_bf16 v[148:151], v[200:203], v[240:243], v[148:151]\n\t" \
  "s_cmp_lg_u32 s63, 0\n\t" \
  "s_cbranch_scc1 .Lcg1_%=\n\t" \
  "s_swappc_b64 s[60:61], s[56:57]\n\t" \
  "s_branch .Lce1_%=\n\t" \
  ".Lcg1_%=:\n\t" \
  "s_swappc_b64 s[60:61], s[58:59]\n\t" \
  "s_swappc_b64 s[60:61], s[56:57]\n\t" \
  ".Lce1_%=:\n\t" \
  "v_mfma_f32_16x16x32_bf16 v[152:155], v[200:203], v[244:247], v[152:155]\n\t" \
  "v_mfma_f32_16x16x32_bf16 v[156:159], v[204:207], v[228:231], v[156:159]\n\t" \
  "v_mfma_f32_16x16x32_bf16 v[160:163], v[204:207], v[232:235], v[160:163]\n\t" \
  "v_mfma_f32_16x16x32_bf16 v[164:167], v[204:207], v[236:239], v[164:167]\n\t" \
  "v_mfma_f32_16x16x32_bf16 v[168:171], v[204:207], v[240:243], v[168:171]\n\t" \
  "v_mfma_f32_16x16x32_bf16 v[172:175], v[204:207], v[244:247], v[172:175]\n\t" \
  "s_waitcnt lgkmcnt(0)\n\t" \
  "v_mfma_f32_16x16x32_bf16 v[96:99], v[176:179], v[208:211], v[96:99]\n\t" \
  "ds_read_b128 v[228:231], v72 offset:0\n\t" \
  "v_mfma_f32_16x16x32_bf16 v[100:103], v[176:179], v[212:215], v[100:103]\n\t" \
  "ds_read_b128 v[232:235], v72 offset:2048\n\t" \
  "v_mfma_f32_16x16x32_bf16 v[104:107], v[176:179], v[216:219], v[104:107]\n\t" \
  "ds_read_b128 v[236:239], v72 offset:4096\n\t" \
  "v_mfma_f32_16x16x32_bf16 v[108:111], v[176:179], v[220:223], v[108:111]\n\t" \
  "ds_read_b128 v[240:243], v72 offset:6144\n\t" \
  "v_mfma_f32_16x16x32_bf16 v[112:115], v[176:179], v[224:227], v[112:115]\n\t" \
  "ds_read_b128 v[244:247], v72 offset:8192\n\t" \
  "v_mfma_f32_16x16x32_bf16 v[116:119], v[180:183], v[208:211], v[116:119]\n\t" \
  "ds_read_b64_tr_b16 v[192:193], v248 offset:8192\n\t" \
  "v_mfma_f32_16x16x32_bf16 v[120:123], v[180:183], v[212:215], v[120:123]\n\t" \
  "ds_read_b64_tr_b16 v[194:195], v248 offset:9216\n\t" \
  "s_mul_i32 s71, s96, 5\n\t" \
  "v_mfma_f32_16x16x32_bf16 v[124:127], v[180:183], v[216:219], v[124:127]\n\t" \
  "ds_read_b64_tr_b16 v[196:197], v249 offset:8192\n\t" \
  "s_add_u32 s71, s71, 86016\n\t" \
  "v_mfma_f32_16x16x32_bf16 v[128:131], v[180:183], v[220:223], v[128:131]\n\t" \
  "ds_read_b64_tr_b16 v[198:199], v249 offset:9216\n\t" \
  "s_cmp_lg_u32 s97, 0\n\t" \
  "s_cselect_b32 s72, 10240, 0\n\t" \
  "s_cselect_b32 s78, 0, 10240\n\t" \
  "v_mfma_f32_16x16x32_bf16 v[132:135], v[180:183], v[224:227], v[132:135]\n\t" \
  "ds_read_b64_tr_b16 v[200:201], v250 offset:8192\n\t" \
  "s_add_u32 s72, s72, s71\n\t" \
  "v_mfma_f32_16x16x32_bf16 v[136:139], v[184:187], v[208:211], v[136:139]\n\t" \
  "ds_read_b64_tr_b16 v[202:203], v250 offset:9216\n\t" \
  "s_add_u32 s78, s78, s71\n\t" \
  "v_mfma_f32_16x16x32_bf16 v[140:143], v[184:187], v[212:215], v[140:143]\n\t" \
  "ds_read_b64_tr_b16 v[204:205], v251 offset:8192\n\t" \
  "v_mfma_f32_16x16x32_bf16 v[144:147], v[184:187], v[216:219], v[144:147]\n\t" \
  "ds_read_b64_tr_b16 v[206:207], v251 offset:9216\n\t" \
  "v_mfma_f32_16x16x32_bf16 v[148:151], v[184:187], v[220:223], v[148:151]\n\t" \
  "v_mfma_f32_16x16x32_bf16 v[152:155], v[184:187], v[224:227], v[152:155]\n\t" \
  "v_add_u32 v250, s72, %[qw]\n\t" \
  "v_mfma_f32_16x16x32_bf16 v[156:159], v[188:191], v[208:211], v[156:159]\n\t" \
  "v_add_u32 v248, s72, %[l16]\n\t" \
  "v_mfma_f32_16x16x32_bf16 v[160:163], v[188:191], v[212:215], v[160:163]\n\t" \
  "v_add_u32 v249, s78, %[l16]\n\t" \
  "v_mfma_f32_16x16x32_bf16 v[164:167], v[188:191], v[216:219], v[164:167]\n\t" \
  "v_mfma_f32_16x16x32_bf16 v[168:171], v[188:191], v[220:223], v[168:171]\n\t" \
  "v_mfma_f32_16x16x32_bf16 v[172:175], v[188:191], v[224:227], v[172:175]\n\t" \
  "s_waitcnt lgkmcnt(0)\n\t" \
  "s_barrier\n\t" \
  "v_mfma_f32_16x16x32_bf16 v[96:99], v[192:195], v[228:231], v[96:99]\n\t" \
  "s_swappc_b64 s[60:61], s[58:59]\n\t" \
  "s_cmp_lg_u32 s63, 0\n\t" \
  "s_cbranch_scc0 .Lce2_%=\n\t" \
  "s_swappc_b64 s[60:61], s[56:57]\n\t" \
  ".Lce2_%=:\n\t" \
  "v_mfma_f32_16x16x32_bf16 v[100:103], v[192:195], v[232:235], v[100:103]\n\t" \
  "v_mfma_f32_16x16x32_bf16 v[104:107], v[192:195], v[236:239], v[104:107]\n\t" \
  "v_mfma_f32_16x16x32_bf16 v[108:111], v[192:195], v[240:243], v[108:111]\n\t" \
  "v_mfma_f32_16x16x32_bf16 v[112:115], v[192:195], v[244:247], v[112:115]\n\t" \
  "v_mfma_f32_16x16x32_bf16 v[116:119], v[196:199], v[228:231], v[116:119]\n\t" \
  "v_mfma_f32_16x16x32_bf16 v[120:123], v[196:199], v[232:235], v[120:123]\n\t" \
  "v_mfma_f32_16x16x32_bf16 v[124:127], v[196:199], v[236:239], v[124:127]\n\t" \
  "v_mfma_f32_16x16x32_bf16 v[128:131], v[196:199], v[240:243], v[128:131]\n\t" \
  "v_mul_f32 v96, s80, v96\n\t" \
  "v_mul_f32 v97, s80, v97\n\t" \
  "v_mul_f32 v98, s80, v98\n\t" \
  "v_mfma_f32_16x16x32_bf16 v[132:135], v[196:199], v[244:247], v[132:135]\n\t" \
  "v_mul_f32 v99, s80, v99\n\t" \
  "v_cvt_pk_bf16_f32 v96, v96, v97\n\t" \
  "v_mfma_f32_16x16x32_bf16 v[136:139], v[200:203], v[228:231], v[136:139]\n\t" \
  "v_cvt_pk_bf16_f32 v97, v98, v99\n\t" \
  "v_mfma_f32_16x16x32_bf16 v[140:143], v[200:203], v[232:235], v[140:143]\n\t" \
  "ds_write_b64 v250, v[96:97] offset:0\n\t" \
  "v_mul_f32 v100, s80, v100\n\t" \
  "v_mul_f32 v101, s80, v101\n\t" \
  "v_mul_f32 v102, s80, v102\n\t" \
  "v_mfma_f32_16x16x32_bf16 v[144:147], v[200:203], v[236:239], v[144:147]\n\t" \
  "v_mul_f32 v103, s80, v103\n\t" \
  "v_cvt_pk_bf16_f32 v100, v100, v101\n\t" \
  "v_mfma_f32_16x16x32_bf16 v[148:151], v[200:203], v[240:243], v[148:151]\n\t" \
  "v_cvt_pk_bf16_f32 v101, v102, v103\n\t" \
  "v_mfma_f32_16x16x32_bf16 v[152:155], v[200:203], v[244:247], v[152:155]\n\t" \
  "ds_write_b64 v250, v[100:101] offset:1024\n\t" \
  "v_mul_f32 v104, s80, v104\n\t" \
  "v_mul_f32 v105, s80, v105\n\t" \
  "v_mul_f32 v106, s80, v106\n\t" \
  "v_mfma_f32_16x16x32_bf16 v[156:159], v[204:207], v[228:231], v[156:159]\n\t" \
  "v_mul_f32 v107, s80, v107\n\t" \
  "v_cvt_pk_bf16_f32 v104, v104, v105\n\t" \
  "v_mfma_f32_16x16x32_bf16 v[160:163], v[204:207], v[232:235], v[160:163]\n\t" \
  "v_cvt_pk_bf16_f32 v105, v106, v107\n\t" \
  "v_mfma_f32_16x16x32_bf16 v[164:167], v[204:207], v[236:239], v[164:167]\n\t" \
  "ds_write_b64 v250, v[104:105] offset:2048\n\t" \
  "v_mul_f32 v108, s80, v108\n\t" \
  "v_mul_f32 v109, s80, v109\n\t" \
  "v_mul_f32 v110, s80, v110\n\t" \
  "v_mfma_f32_16x16x32_bf16 v[168:171], v[204:207], v[240:243], v[168:171]\n\t" \
  "v_mul_f32 v111, s80, v111\n\t" \
  "v_cvt_pk_bf16_f32 v108, v108, v109\n\t" \
  "v_mfma_f32_16x16x32_bf16 v[172:175], v[204:207], v[244:247], v[172:175]\n\t" \
  "v_cvt_pk_bf16_f32 v109, v110, v111\n\t" \
  "ds_write_b64 v250, v[108:109] offset:3072\n\t" \
  "v_pk_mul_f32 v[112:113], v[112:113], s[80:81]\n\t" \
  "v_pk_mul_f32 v[114:115], v[114:115], s[80:81]\n\t" \
  "v_cvt_pk_bf16_f32 v112, v112, v113\n\t" \
  "v_cvt_pk_bf16_f32 v113, v114, v115\n\t" \
  "v_pk_mul_f32 v[116:117], v[116:117], s[80:81]\n\t" \
  "v_pk_mul_f32 v[118:119], v[118:119], s[80:81]\n\t" \
  "s_nop 0\n\t" \
  "ds_write_b64 v250, v[112:113] offset:4096\n\t" \
  "v_cvt_pk_bf16_f32 v116, v116, v117\n\t" \
  "v_cvt_pk_bf16_f32 v117, v118, v119\n\t" \
  "v_pk_mul_f32 v[120:121], v[120:121], s[80:81]\n\t" \
  "v_pk_mul_f32 v[122:123], v[122:123], s[80:81]\n\t" \
  "s_nop 0\n\t" \
  "ds_write_b64 v250, v[116:117] offset:512\n\t" \
  "v_cvt_pk_bf16_f32 v120, v120, v121\n\t" \
  "v_cvt_pk_bf16_f32 v121, v122, v123\n\t" \
  "v_pk_mul_f32 v[124:125], v[124:125], s[80:81]\n\t" \
  "v_pk_mul_f32 v[126:127], v[126:127], s[80:81]\n\t" \
  "s_nop 0\n\t" \
  "ds_write_b64 v250, v[120:121] offset:1536\n\t" \
  "v_cvt_pk_bf16_f32 v124, v124, v125\n\t" \
  "v_cvt_pk_bf16_f32 v125, v126, v127\n\t" \
  "v_pk_mul_f32 v[128:129], v[128:129], s[80:81]\n\t" \
  "v_pk_mul_f32 v[130:131], v[130:131], s[80:81]\n\t" \
  "s_nop 0\n\t" \
  "ds_write_b64 v250, v[124:125] offset:2560\n\t" \
  "v_cvt_pk_bf16_f32 v128, v128, v129\n\t" \
  "v_cvt_pk_bf16_f32 v129, v130, v131\n\t" \
  "v_pk_mul_f32 v[132:133], v[132:133], s[80:81]\n\t" \
  "v_pk_mul_f32 v[134:135], v[134:135], s[80:81]\n\t" \
  "s_nop 0\n\t" \
  "ds_write_b64 v250, v[128:129] offset:3584\n\t" \
  "v_cvt_pk_bf16_f32 v132, v132, v133\n\t" \
  "v_cvt_pk_bf16_f32 v133, v134, v135\n\t" \
  "v_pk_mul_f32 v[136:137], v[136:137], s[80:81]\n\t" \
  "v_pk_mul_f32 v[138:139], v[138:139], s[80:81]\n\t" \
  "s_nop 0\n\t" \
  "ds_write_b64 v250, v[132:133] offset:4608\n\t" \
  "v_cvt_pk_bf16_f32 v136, v136, v137\n\t" \
  "v_cvt_pk_bf16_f32 v137, v138, v139\n\t" \
  "v_pk_mul_f32 v[140:141], v[140:141], s[80:81]\n\t" \
  "v_pk_mul_f32 v[142:143], v[142:143], s[80:81]\n\t" \
  "s_nop 0\n\t" \
  "ds_write_b64 v250, v[136:137] offset:5120\n\t" \
  "v_cvt_pk_bf16_f32 v140, v140, v141\n\t" \
  "v_cvt_pk_bf16_f32 v141, v142, v143\n\t" \
  "v_pk_mul_f32 v[144:145], v[144:145], s[80:81]\n\t" \
  "v_pk_mul_f32 v[146:147], v[146:147], s[80:81]\n\t" \
  "s_nop 0\n\t" \
  "ds_write_b64 v250, v[140:141] offset:6144\n\t" \
  "v_cvt_pk_bf16_f32 v144, v144, v145\n\t" \
  "v_cvt_pk_bf16_f32 v145, v146, v147\n\t" \
  "v_pk_mul_f32 v[148:149], v[148:149], s[80:81]\n\t" \
  "v_pk_mul_f32 v[150:151], v[150:151], s[80:81]\n\t" \
  "s_nop 0\n\t" \
  "ds_write_b64 v250, v[144:145] offset:7168\n\t" \
  "v_cvt_pk_bf16_f32 v148, v148, v149\n\t" \
  "v_cvt_pk_bf16_f32 v149, v150, v151\n\t" \
  "v_pk_mul_f32 v[152:153], v[152:153], s[80:81]\n\t" \
  "v_pk_mul_f32 v[154:155], v[154:155], s[80:81]\n\t" \
  "s_nop 0\n\t" \
  "ds_write_b64 v250, v[148:149] offset:8192\n\t" \
  "v_cvt_pk_bf16_f32 v152, v152, v153\n\t" \
  "v_cvt_pk_bf16_f32 v153, v154, v155\n\t" \
  "v_pk_mul_f32 v[156:157], v[156:157], s[80:81]\n\t" \
  "v_pk_mul_f32 v[158:159], v[158:159], s[80:81]\n\t" \
  "s_nop 0\n\t" \
  "ds_write_b64 v250, v[152:153] offset:9216\n\t" \
  "v_cvt_pk_bf16_f32 v156, v156, v157\n\t" \
  "v_cvt_pk_bf16_f32 v157, v158, v159\n\t" \
  "v_pk_mul_f32 v[160:161], v[160:161], s[80:81]\n\t" \
  "v_pk_mul_f32 v[162:163], v[162:163], s[80:81]\n\t" \
  "s_nop 0\n\t" \
  "ds_write_b64 v250, v[156:157] offset:5632\n\t" \
  "v_cvt_pk_bf16_f32 v160, v160, v161\n\t" \
  "v_cvt_pk_bf16_f32 v161, v162, v163\n\t" \
  "v_pk_mul_f32 v[164:165], v[164:165], s[80:81]\n\t" \
  "v_pk_mul_f32 v[166:167], v[166:167], s[80:81]\n\t" \
  "s_nop 0\n\t" \
  "ds_write_b64 v250, v[160:161] offset:6656\n\t" \
  "v_cvt_pk_bf16_f32 v164, v164, v165\n\t" \
  "v_cvt_pk_bf16_f32 v165, v166, v167\n\t" \
  "v_pk_mul_f32 v[168:169], v[168:169], s[80:81]\n\t" \
  "v_pk_mul_f32 v[170:171], v[170:171], s[80:81]\n\t" \
  "s_nop 0\n\t" \
  "ds_write_b64 v250, v[164:165] offset:7680\n\t" \
  "v_cvt_pk_bf16_f32 v168, v168, v169\n\t" \
  "v_cvt_pk_bf16_f32 v169, v170, v171\n\t" \
  "v_pk_mul_f32 v[172:173], v[172:173], s[80:81]\n\t" \
  "v_pk_mul_f32 v[174:175], v[174:175], s[80:81]\n\t" \
  "s_nop 0\n\t" \
  "ds_write_b64 v250, v[168:169] offset:8704\n\t" \
  "v_cvt_pk_bf16_f32 v172, v172, v173\n\t" \
  "v_cvt_pk_bf16_f32 v173, v174, v175\n\t" \
  "ds_write_b64 v250, v[172:173] offset:9728\n\t" \
  "s_cmp_lg_u32 s63, 0\n\t" \
  "s_cselect_b32 s75, 1, 3\n\t" \
  "s_cselect_b32 s76, 2, 2\n\t" \
  "s_cselect_b32 s77, 3, 4\n\t" \
  "s_cselect_b32 s80, 4, 0\n\t" \
  "s_cselect_b32 s81, 0, 1\n\t" \
  "s_lshl_b32 s75, s75, 15\n\t" \
  "s_add_u32 s75, s75, %[w8k]\n\t" \
  "s_lshl_b32 s76, s76, 15\n\t" \
  "s_add_u32 s76, s76, %[w8k]\n\t" \
  "s_lshl_b32 s77, s77, 15\n\t" \
  "s_add_u32 s77, s77, %[w8k]\n\t" \
  "s_lshl_b32 s80, s80, 15\n\t" \
  "s_add_u32 s80, s80, %[w8k]\n\t" \
  "s_lshl_b32 s81, s81, 15\n\t" \
  "s_add_u32 s81, s81, %[w8k]\n\t" \
  "s_waitcnt vmcnt(16) lgkmcnt(0)\n\t" \
  "s_barrier\n\t" \
  "ds_read_b128 v[56:59], v248 offset:0\n\t" \
  "ds_read_b128 v[60:63], v248 offset:1024\n\t" \
  "ds_read_b128 v[64:67], v248 offset:2048\n\t" \
  "ds_read_b128 v[68:71], v248 offset:3072\n\t" \
  "ds_read_b128 v[72:75], v248 offset:4096\n\t" \
  "ds_read_b128 a[180:183], v248 offset:5120\n\t" \
  "ds_read_b128 a[184:187], v248 offset:6144\n\t" \
  "ds_read_b128 a[188:191], v248 offset:7168\n\t" \
  "ds_read_b128 a[192:195], v248 offset:8192\n\t" \
  "ds_read_b128 a[196:199], v248 offset:9216\n\t" \
  "ds_read_b128 a[200:203], v249 offset:0\n\t" \
  "ds_read_b128 a[204:207], v249 offset:1024\n\t" \
  "ds_read_b128 a[208:211], v249 offset:2048\n\t" \
  "ds_read_b128 a[212:215], v249 offset:3072\n\t" \
  "ds_read_b128 a[216:219], v249 offset:4096\n\t" \
  "ds_read_b128 a[220:223], v249 offset:5120\n\t" \
  "ds_read_b128 a[224:227], v249 offset:6144\n\t" \
  "ds_read_b128 a[228:231], v249 offset:7168\n\t" \
  "ds_read_b128 a[232:235], v249 offset:8192\n\t" \
  "ds_read_b128 a[236:239], v249 offset:9216\n\t" \
  "v_add_u32 v220, s65, %[kb]\n\t" \
  "v_xor_b32 v221, 64, v220\n\t" \
  "v_xor_b32 v222, 128, v220\n\t" \
  "v_xor_b32 v223, 192, v220\n\t" \
  "ds_read_b128 v[156:159], v220\n\t" \
  "ds_read_b128 v[160:163], v221\n\t" \
  "ds_read_b128 v[164:167], v222\n\t" \
  "ds_read_b128 v[168:171], v223\n\t" \
  "ds_read_b128 v[172:175], v220 offset:4096\n\t" \
  "ds_read_b128 v[176:179], v221 offset:4096\n\t" \
  "ds_read_b128 v[180:183], v222 offset:4096\n\t" \
  "ds_read_b128 v[184:187], v223 offset:4096\n\t" \
  "s_add_u32 s69, s65, s96\n\t" \
  "s_add_u32 s65, s65, s62\n\t" \
  "s_sub_u32 s79, s65, 163840\n\t" \
  "s_cselect_b32 s65, s65, s79\n\t" \
  "s_waitcnt lgkmcnt(0)\n\t" \
  "s_barrier\n\t" \
  "v_mfma_f32_16x16x32_bf16 v[96:99], v[156:159], v[56:59], 0\n\t" \
  "s_cmp_lg_u32 s63, 0\n\t" \
  "s_cbranch_scc1 .Lcg3_%=\n\t" \
  "s_swappc_b64 s[60:61], s[56:57]\n\t" \
  "s_branch .Lce3_%=\n\t" \
  ".Lcg3_%=:\n\t" \
  "s_swappc_b64 s[60:61], s[58:59]\n\t" \
  ".Lce3_%=:\n\t" \
  "v_mfma_f32_16x16x32_bf16 v[96:99], v[160:163], a[180:183], v[96:99]\n\t" \
  "v_mfma_f32_16x16x32_bf16 v[96:99], v[164:167], a[200:203], v[96:99]\n\t" \
  "v_mfma_f32_16x16x32_bf16 v[96:99], v[168:171], a[220:223], v[96:99]\n\t" \
  "v_mfma_f32_16x16x32_bf16 v[116:119], v[172:175], v[56:59], 0\n\t" \
  "v_mfma_f32_16x16x32_bf16 v[116:119], v[176:179], a[180:183], v[116:119]\n\t" \
  "v_mfma_f32_16x16x32_bf16 v[116:119], v[180:183], a[200:203], v[116:119]\n\t" \
  "v_mfma_f32_16x16x32_bf16 v[116:119], v[184:187], a[220:223], v[116:119]\n\t" \
  "v_mfma_f32_16x16x32_bf16 v[100:103], v[156:159], v[60:63], 0\n\t" \
  "s_cmp_lg_u32 s63, 0\n\t" \
  "s_cbranch_scc0 .Lce4_%=\n\t" \
  "s_swappc_b64 s[60:61], s[56:57]\n\t" \
  ".Lce4_%=:\n\t" \
  "v_mfma_f32_16x16x32_bf16 v[100:103], v[160:163], a[184:187], v[100:103]\n\t" \
  "v_mfma_f32_16x16x32_bf16 v[100:103], v[164:167], a[204:207], v[100:103]\n\t" \
  "v_mfma_f32_16x16x32_bf16 v[100:103], v[168:171], a[224:227], v[100:103]\n\t" \
  "v_mfma_f32_16x16x32_bf16 v[120:123], v[172:175], v[60:63], 0\n\t" \
  "v_mfma_f32_16x16x32_bf16 v[120:123], v[176:179], a[184:187], v[120:123]\n\t" \
  "v_mfma_f32_16x16x32_bf16 v[120:123], v[180:183], a[204:207], v[120:123]\n\t" \
  "v_max3_f32 v228, v96, v97, v98\n\t" \
  "v_mfma_f32_16x16x32_bf16 v[120:123], v[184:187], a[224:227], v[120:123]\n\t" \
  "v_max3_f32 v233, v99, v116, v117\n\t" \
  "v_mfma_f32_16x16x32_bf16 v[104:107], v[156:159], v[64:67], 0\n\t" \
  "s_cmp_lg_u32 s63, 0\n\t" \
  "s_cbranch_scc0 .Lce5_%=\n\t" \
  "s_swappc_b64 s[60:61], s[58:59]\n\t" \
  ".Lce5_%=:\n\t" \
  "v_max3_f32 v228, v228, v118, v119\n\t" \
  "v_mfma_f32_16x16x32_bf16 v[104:107], v[160:163], a[188:191], v[104:107]\n\t" \
  "v_max_f32 v228, v228, v233\n\t" \
  "v_mfma_f32_16x16x32_bf16 v[104:107], v[164:167], a[208:211], v[104:107]\n\t" \
  "v_mfma_f32_16x16x32_bf16 v[104:107], v[168:171], a[228:231], v[104:107]\n\t" \
  "v_mfma_f32_16x16x32_bf16 v[124:127], v[172:175], v[64:67], 0\n\t" \
  "v_mfma_f32_16x16x32_bf16 v[124:127], v[176:179], a[188:191], v[124:127]\n\t" \
  "v_mfma_f32_16x16x32_bf16 v[124:127], v[180:183], a[208:211], v[124:127]\n\t" \
  "v_max3_f32 v229, v100, v101, v102\n\t" \
  "v_mfma_f32_16x16x32_bf16 v[124:127], v[184:187], a[228:231], v[124:127]\n\t" \
  "v_max3_f32 v234, v103, v120, v121\n\t" \
  "v_mfma_f32_16x16x32_bf16 v[108:111], v[156:159], v[68:71], 0\n\t" \
  "s_cmp_lg_u32 s63, 0\n\t" \
  "s_cbranch_scc0 .Lce6_%=\n\t" \
  "s_swappc_b64 s[60:61], s[56:57]\n\t" \
  ".Lce6_%=:\n\t" \
  "v_max3_f32 v229, v229, v122, v123\n\t" \
  "v_mfma_f32_16x16x32_bf16 v[108:111], v[160:163], a[192:195], v[108:111]\n\t" \
  "v_max_f32 v229, v229, v234\n\t" \
  "v_mfma_f32_16x16x32_bf16 v[108:111], v[164:167], a[212:215], v[108:111]\n\t" \
  "v_mfma_f32_16x16x32_bf16 v[108:111], v[168:171], a[232:235], v[108:111]\n\t" \
  "v_mfma_f32_16x16x32_bf16 v[128:131], v[172:175], v[68:71], 0\n\t" \
  "v_mfma_f32_16x16x32_bf16 v[128:131], v[176:179], a[192:195], v[128:131]\n\t" \
  "v_mfma_f32_16x16x32_bf16 v[128:131], v[180:183], a[212:215], v[128:131]\n\t" \
  "v_max3_f32 v230, v104, v105, v106\n\t" \
  "v_add_u32 v220, s65, %[kb]\n\t" \
  "v_xor_b32 v221, 64, v220\n\t" \
  "v_xor_b32 v222, 128, v220\n\t" \
  "v_xor_b32 v223, 192, v220\n\t" \
  "v_mfma_f32_16x16x32_bf16 v[128:131], v[184:187], a[232:235], v[128:131]\n\t" \
  "v_max3_f32 v233, v107, v124, v125\n\t" \
  "v_mfma_f32_16x16x32_bf16 v[112:115], v[156:159], v[72:75], 0\n\t" \
  "s_add_u32 s66, s65, s62\n\t" \
  "s_sub_u32 s79, s66, 163840\n\t" \
  "s_cselect_b32 s66, s66, s79\n\t" \
  "v_max3_f32 v230, v230, v126, v127\n\t" \
  "v_mfma_f32_16x16x32_bf16 v[112:115], v[160:163], a[196:199], v[112:115]\n\t" \
  "v_max_f32 v230, v230, v233\n\t" \
  "v_mfma_f32_16x16x32_bf16 v[112:115], v[164:167], a[216:219], v[112:115]\n\t" \
  "v_mfma_f32_16x16x32_bf16 v[112:115], v[168:171], a[236:239], v[112:115]\n\t" \
  "v_mfma_f32_16x16x32_bf16 v[132:135], v[172:175], v[72:75], 0\n\t" \
  "v_mfma_f32_16x16x32_bf16 v[132:135], v[176:179], a[196:199], v[132:135]\n\t" \
  "v_mfma_f32_16x16x32_bf16 v[132:135], v[180:183], a[216:219], v[132:135]\n\t" \
  "v_max3_f32 v231, v108, v109, v110\n\t" \
  "v_mfma_f32_16x16x32_bf16 v[132:135], v[184:187], a[236:239], v[132:135]\n\t" \
  "v_max3_f32 v234, v111, v128, v129\n\t" \
  "v_max3_f32 v231, v231, v130, v131\n\t" \
  "v_max_f32 v231, v231, v234\n\t" \
  "s_cmp_lg_u32 s63, 0\n\t" \
  "s_cbranch_scc1 .Lk1w_%=\n\t" \
  "s_waitcnt vmcnt(16)\n\t" \
  ".Lk1w_%=:\n\t" \
  "ds_read_b128 v[156:159], v220\n\t" \
  "ds_read_b128 v[160:163], v221\n\t" \
  "ds_read_b128 v[164:167], v222\n\t" \
  "ds_read_b128 v[168:171], v223\n\t" \
  "ds_read_b128 v[172:175], v220 offset:4096\n\t" \
  "ds_read_b128 v[176:179], v221 offset:4096\n\t" \
  "ds_read_b128 v[180:183], v222 offset:4096\n\t" \
  "ds_read_b128 v[184:187], v223 offset:4096\n\t" \
  "v_max3_f32 v232, v112, v113, v114\n\t" \
  "v_max3_f32 v233, v115, v132, v133\n\t" \
  "v_max3_f32 v232, v232, v134, v135\n\t" \
  "v_max_f32 v232, v232, v233\n\t" \
  "v_mov_b32 v233, v228\n\t" \
  "v_mov_b32 v234, v229\n\t" \
  "v_mov_b32 v235, v230\n\t" \
  "v_mov_b32 v236, v231\n\t" \
  "v_mov_b32 v237, v232\n\t" \
  "s_nop 1\n\t" \
  "v_permlane16_swap_b32 v228, v233\n\t" \
  "v_permlane16_swap_b32 v229, v234\n\t" \
  "v_permlane16_swap_b32 v230, v235\n\t" \
  "v_permlane16_swap_b32 v231, v236\n\t" \
  "v_permlane16_swap_b32 v232, v237\n\t" \
  "s_nop 1\n\t" \
  "v_max_f32 v228, v228, v233\n\t" \
  "v_max_f32 v229, v229, v234\n\t" \
  "v_max_f32 v230, v230, v235\n\t" \
  "v_max_f32 v231, v231, v236\n\t" \
  "v_max_f32 v232, v232, v237\n\t" \
  "v_mov_b32 v233, v228\n\t" \
  "v_mov_b32 v234, v229\n\t" \
  "v_mov_b32 v235, v230\n\t" \
  "v_mov_b32 v236, v231\n\t" \
  "v_mov_b32 v237, v232\n\t" \
  "s_nop 1\n\t" \
  "v_permlane32_swap_b32 v228, v233\n\t" \
  "v_permlane32_swap_b32 v229, v234\n\t" \
  "v_permlane32_swap_b32 v230, v235\n\t" \
  "v_permlane32_swap_b32 v231, v236\n\t" \
  "v_permlane32_swap_b32 v232, v237\n\t" \
  "s_nop 1\n\t" \
  "v_max_f32 v228, v228, v233\n\t" \
  "v_max_f32 v229, v229, v234\n\t" \
  "v_max_f32 v230, v230, v235\n\t" \
  "v_max_f32 v231, v231, v236\n\t" \
  "v_max_f32 v232, v232, v237\n\t" \
  "v_sub_f32 v96, v96, v228\n\t" \
  "v_sub_f32 v97, v97, v228\n\t" \
  "v_sub_f32 v98, v98, v228\n\t" \
  "v_sub_f32 v99, v99, v228\n\t" \
  "v_sub_f32 v116, v116, v228\n\t" \
  "v_sub_f32 v117, v117, v228\n\t" \
  "v_sub_f32 v118, v118, v228\n\t" \
  "v_sub_f32 v119, v119, v228\n\t" \
  "v_sub_f32 v76, 0, v228\n\t" \
  "v_sub_f32 v77, 0, v228\n\t" \
  "v_sub_f32 v78, 0, v228\n\t" \
  "v_sub_f32 v79, 0, v228\n\t" \
  "v_sub_f32 v100, v100, v229\n\t" \
  "v_sub_f32 v101, v101, v229\n\t" \
  "v_sub_f32 v102, v102, v229\n\t" \
  "v_sub_f32 v103, v103, v229\n\t" \
  "v_sub_f32 v120, v120, v229\n\t" \
  "v_sub_f32 v121, v121, v229\n\t" \
  "v_sub_f32 v122, v122, v229\n\t" \
  "v_sub_f32 v123, v123, v229\n\t" \
  "v_sub_f32 v80, 0, v229\n\t" \
  "v_sub_f32 v81, 0, v229\n\t" \
  "v_sub_f32 v82, 0, v229\n\t" \
  "v_sub_f32 v83, 0, v229\n\t" \
  "v_sub_f32 v104, v104, v230\n\t" \
  "v_sub_f32 v105, v105, v230\n\t" \
  "v_sub_f32 v106, v106, v230\n\t" \
  "v_sub_f32 v107, v107, v230\n\t" \
  "v_sub_f32 v124, v124, v230\n\t" \
  "v_sub_f32 v125, v125, v230\n\t" \
  "v_sub_f32 v126, v126, v230\n\t" \
  "v_sub_f32 v127, v127, v230\n\t" \
  "v_sub_f32 v84, 0, v230\n\t" \
  "v_sub_f32 v85, 0, v230\n\t" \
  "v_sub_f32 v86, 0, v230\n\t" \
  "v_sub_f32 v87, 0, v230\n\t" \
  "v_sub_f32 v108, v108, v231\n\t" \
  "v_sub_f32 v109, v109, v231\n\t" \
  "v_sub_f32 v110, v110, v231\n\t" \
  "v_sub_f32 v111, v111, v231\n\t" \
  "v_sub_f32 v128, v128, v231\n\t" \
  "v_sub_f32 v129, v129, v231\n\t" \
  "v_sub_f32 v130, v130, v231\n\t" \
  "v_sub_f32 v131, v131, v231\n\t" \
  "v_sub_f32 v88, 0, v231\n\t" \
  "v_sub_f32 v89, 0, v231\n\t" \
  "v_sub_f32 v90, 0, v231\n\t" \
  "v_sub_f32 v91, 0, v231\n\t" \
  "v_sub_f32 v112, v112, v232\n\t" \
  "v_sub_f32 v113, v113, v232\n\t" \
  "v_sub_f32 v114, v114, v232\n\t" \
  "v_sub_f32 v115, v115, v232\n\t" \
  "v_sub_f32 v132, v132, v232\n\t" \
  "v_sub_f32 v133, v133, v232\n\t" \
  "v_sub_f32 v134, v134, v232\n\t" \
  "v_sub_f32 v135, v135, v232\n\t" \
  "v_sub_f32 v92, 0, v232\n\t" \
  "v_sub_f32 v93, 0, v232\n\t" \
  "v_sub_f32 v94, 0, v232\n\t" \
  "v_sub_f32 v95, 0, v232\n\t" \
  "v_exp_f32 v96, v96\n\t" \
  "v_exp_f32 v97, v97\n\t" \
  "v_exp_f32 v98, v98\n\t" \
  "v_exp_f32 v99, v99\n\t" \
  "v_exp_f32 v116, v116\n\t" \
  "v_exp_f32 v117, v117\n\t" \
  "v_exp_f32 v118, v118\n\t" \
  "v_exp_f32 v119, v119\n\t" \
  "v_exp_f32 v100, v100\n\t" \
  "v_exp_f32 v101, v101\n\t" \
  "v_exp_f32 v102, v102\n\t" \
  "v_exp_f32 v103, v103\n\t" \
  "v_exp_f32 v120, v120\n\t" \
  "v_exp_f32 v121, v121\n\t" \
  "v_exp_f32 v122, v122\n\t" \
  "v_exp_f32 v123, v123\n\t" \
  "v_exp_f32 v104, v104\n\t" \
  "v_exp_f32 v105, v105\n\t" \
  "v_exp_f32 v106, v106\n\t" \
  "v_exp_f32 v107, v107\n\t" \
  "v_exp_f32 v124, v124\n\t" \
  "v_exp_f32 v125, v125\n\t" \
  "v_exp_f32 v126, v126\n\t" \
  "v_exp_f32 v127, v127\n\t" \
  "v_cvt_pk_bf16_f32 v136, v96, v97\n\t" \
  "v_cvt_pk_bf16_f32 v137, v98, v99\n\t" \
  "v_cvt_pk_bf16_f32 v138, v116, v117\n\t" \
  "v_cvt_pk_bf16_f32 v139, v118, v119\n\t" \
  "v_cvt_pk_bf16_f32 v140, v100, v101\n\t" \
  "v_cvt_pk_bf16_f32 v141, v102, v103\n\t" \
  "v_cvt_pk_bf16_f32 v142, v120, v121\n\t" \
  "v_cvt_pk_bf16_f32 v143, v122, v123\n\t" \
  "v_cvt_pk_bf16_f32 v144, v104, v105\n\t" \
  "v_cvt_pk_bf16_f32 v145, v106, v107\n\t" \
  "v_cvt_pk_bf16_f32 v146, v124, v125\n\t" \
  "v_cvt_pk_bf16_f32 v147, v126, v127\n\t" \
  "s_mov_b32 s57, 0\n\t" \
  "s_add_u32 s92, s92, 1395\n\t" \
  "s_cmp_lg_u32 s63, 0\n\t" \
  "s_cbranch_scc1 .LloopG_%=\n\t" \
  ".LloopM_%=:\n\t" \
  "s_waitcnt lgkmcnt(0)\n\t" \
  "v_mfma_f32_16x16x32_bf16 v[96:99], v[156:159], v[56:59], v[76:79]\n\t" \
  "s_mov_b32 m0, s95\n\t" \
  "s_xor_b32 s71, s68, s99\n\t" \
  "buffer_load_dwordx4 %[vo0], %[srdV], s71 offen nt lds\n\t" \
  "v_mfma_f32_16x16x32_bf16 v[96:99], v[160:163], a[180:183], v[96:99]\n\t" \
  "v_exp_f32 v108, v108\n\t" \
  "v_mfma_f32_16x16x32_bf16 v[96:99], v[164:167], a[200:203], v[96:99]\n\t" \
  "v_exp_f32 v109, v109\n\t" \
  "v_mfma_f32_16x16x32_bf16 v[96:99], v[168:171], a[220:223], v[96:99]\n\t" \
  "s_add_u32 s68, s68, 8192\n\t" \
  "buffer_load_dwordx4 v255, %[srdV], s71 offen offset:1024 nt lds\n\t" \
  "v_add_u32 v220, s66, %[vb]\n\t" \
  "v_mfma_f32_16x16x32_bf16 v[116:119], v[172:175], v[56:59], v[76:79]\n\t" \
  "v_exp_f32 v110, v110\n\t" \
  "v_mfma_f32_16x16x32_bf16 v[116:119], v[176:179], a[180:183], v[116:119]\n\t" \
  "v_exp_f32 v111, v111\n\t" \
  "v_mfma_f32_16x16x32_bf16 v[116:119], v[180:183], a[200:203], v[116:119]\n\t" \
  "buffer_load_dwordx4 %[vo0], %[srdV], s71 offen offset:2048 nt lds\n\t" \
  "v_xor_b32 v221, 32, v220\n\t" \
  "v_xor_b32 v222, 64, v220\n\t" \
  "v_mfma_f32_16x16x32_bf16 v[116:119], v[184:187], a[220:223], v[116:119]\n\t" \
  "v_exp_f32 v128, v128\n\t" \
  "v_mfma_f32_16x16x32_bf16 v[100:103], v[156:159], v[60:63], v[80:83]\n\t" \
  "v_exp_f32 v129, v129\n\t" \
  "v_mfma_f32_16x16x32_bf16 v[100:103], v[160:163], a[184:187], v[100:103]\n\t" \
  "buffer_load_dwordx4 v255, %[srdV], s71 offen offset:3072 nt lds\n\t" \
  "v_xor_b32 v223, 96, v220\n\t" \
  "v_mfma_f32_16x16x32_bf16 v[100:103], v[164:167], a[204:207], v[100:103]\n\t" \
  "v_exp_f32 v130, v130\n\t" \
  "v_mfma_f32_16x16x32_bf16 v[100:103], v[168:171], a[224:227], v[100:103]\n\t" \
  "v_exp_f32 v131, v131\n\t" \
  "v_mfma_f32_16x16x32_bf16 v[120:123], v[172:175], v[60:63], v[80:83]\n\t" \
  "s_add_u32 m0, s95, 4096\n\t" \
  "s_or_b32 s71, s71, 4096\n\t" \
  "buffer_load_dwordx4 %[vo0], %[srdV], s71 offen nt lds\n\t" \
  "v_xor_b32 v224, 128, v220\n\t" \
  "v_mfma_f32_16x16x32_bf16 v[120:123], v[176:179], a[184:187], v[120:123]\n\t" \
  "v_exp_f32 v112, v112\n\t" \
  "v_mfma_f32_16x16x32_bf16 v[120:123], v[180:183], a[204:207], v[120:123]\n\t" \
  "v_exp_f32 v113, v113\n\t" \
  "v_mfma_f32_16x16x32_bf16 v[120:123], v[184:187], a[224:227], v[120:123]\n\t" \
  "buffer_load_dwordx4 v255, %[srdV], s71 offen offset:1024 nt lds\n\t" \
  "v_xor_b32 v225, 160, v220\n\t" \
  "v_mfma_f32_16x16x32_bf16 v[104:107], v[156:159], v[64:67], v[84:87]\n\t" \
  "v_xor_b32 v226, 192, v220\n\t" \
  "v_xor_b32 v227, 224, v220\n\t" \
  "v_mfma_f32_16x16x32_bf16 v[104:107], v[160:163], a[188:191], v[104:107]\n\t" \
  "s_waitcnt vmcnt(14)\n\t" \
  "v_exp_f32 v114, v114\n\t" \
  "v_mfma_f32_16x16x32_bf16 v[104:107], v[164:167], a[208:211], v[104:107]\n\t" \
  "ds_read_b64_tr_b16 v[188:189], v220\n\t" \
  "v_cvt_pk_bf16_f32 v148, v108, v109\n\t" \
  "v_mfma_f32_16x16x32_bf16 v[104:107], v[168:171], a[228:231], v[104:107]\n\t" \
  "ds_read_b64_tr_b16 v[190:191], v220 offset:4096\n\t" \
  "v_cvt_pk_bf16_f32 v149, v110, v111\n\t" \
  "v_mfma_f32_16x16x32_bf16 v[124:127], v[172:175], v[64:67], v[84:87]\n\t" \
  "ds_read_b64_tr_b16 v[192:193], v221\n\t" \
  "v_exp_f32 v115, v115\n\t" \
  "v_mfma_f32_16x16x32_bf16 v[124:127], v[176:179], a[188:191], v[124:127]\n\t" \
  "ds_read_b64_tr_b16 v[194:195], v221 offset:4096\n\t" \
  "v_cvt_pk_bf16_f32 v150, v128, v129\n\t" \
  "v_mfma_f32_16x16x32_bf16 v[124:127], v[180:183], a[208:211], v[124:127]\n\t" \
  "ds_read_b64_tr_b16 v[196:197], v222\n\t" \
  "v_cvt_pk_bf16_f32 v151, v130, v131\n\t" \
  "v_mfma_f32_16x16x32_bf16 v[124:127], v[184:187], a[228:231], v[124:127]\n\t" \
  "ds_read_b64_tr_b16 v[198:199], v222 offset:4096\n\t" \
  "v_exp_f32 v132, v132\n\t" \
  "v_mfma_f32_16x16x32_bf16 v[108:111], v[156:159], v[68:71], v[88:91]\n\t" \
  "ds_read_b64_tr_b16 v[200:201], v223\n\t" \
  "v_cvt_pk_bf16_f32 v152, v112, v113\n\t" \
  "v_mfma_f32_16x16x32_bf16 v[108:111], v[160:163], a[192:195], v[108:111]\n\t" \
  "ds_read_b64_tr_b16 v[202:203], v223 offset:4096\n\t" \
  "v_exp_f32 v133, v133\n\t" \
  "v_mfma_f32_16x16x32_bf16 v[108:111], v[164:167], a[212:215], v[108:111]\n\t" \
  "ds_read_b64_tr_b16 v[204:205], v224\n\t" \
  "v_cvt_pk_bf16_f32 v153, v114, v115\n\t" \
  "v_mfma_f32_16x16x32_bf16 v[108:111], v[168:171], a[232:235], v[108:111]\n\t" \
  "ds_read_b64_tr_b16 v[206:207], v224 offset:4096\n\t" \
  "v_exp_f32 v134, v134\n\t" \
  "v_mfma_f32_16x16x32_bf16 v[128:131], v[172:175], v[68:71], v[88:91]\n\t" \
  "ds_read_b64_tr_b16 v[208:209], v225\n\t" \
  "v_exp_f32 v135, v135\n\t" \
  "v_mfma_f32_16x16x32_bf16 v[128:131], v[176:179], a[192:195], v[128:131]\n\t" \
  "ds_read_b64_tr_b16 v[210:211], v225 offset:4096\n\t" \
  "v_cvt_pk_bf16_f32 v154, v132, v133\n\t" \
  "v_mfma_f32_16x16x32_bf16 v[128:131], v[180:183], a[212:215], v[128:131]\n\t" \
  "ds_read_b64_tr_b16 v[212:213], v226\n\t" \
  "v_cvt_pk_bf16_f32 v155, v134, v135\n\t" \
  "v_mfma_f32_16x16x32_bf16 v[128:131], v[184:187], a[232:235], v[128:131]\n\t" \
  "ds_read_b64_tr_b16 v[214:215], v226 offset:4096\n\t" \
  "v_max3_f32 v228, v96, v97, v98\n\t" \
  "v_mfma_f32_16x16x32_bf16 v[112:115], v[156:159], v[72:75], v[92:95]\n\t" \
  "ds_read_b64_tr_b16 v[216:217], v227\n\t" \
  "v_max3_f32 v229, v99, v116, v117\n\t" \
  "v_mfma_f32_16x16x32_bf16 v[112:115], v[160:163], a[196:199], v[112:115]\n\t" \
  "ds_read_b64_tr_b16 v[218:219], v227 offset:4096\n\t" \
  "v_max3_f32 v228, v228, v118, v119\n\t" \
  "v_mfma_f32_16x16x32_bf16 v[112:115], v[164:167], a[216:219], v[112:115]\n\t" \
  "buffer_load_dwordx4 %[vo0], %[srdV], s71 offen offset:2048 nt lds\n\t" \
  "v_max3_f32 v229, v229, v100, v101\n\t" \
  "v_mfma_f32_16x16x32_bf16 v[112:115], v[168:171], a[236:239], v[112:115]\n\t" \
  "v_max3_f32 v228, v228, v102, v103\n\t" \
  "v_max3_f32 v229, v229, v120, v121\n\t" \
  "v_mfma_f32_16x16x32_bf16 v[132:135], v[172:175], v[72:75], v[92:95]\n\t" \
  "v_max3_f32 v228, v228, v122, v123\n\t" \
  "v_max3_f32 v229, v229, v104, v105\n\t" \
  "v_mfma_f32_16x16x32_bf16 v[132:135], v[176:179], a[196:199], v[132:135]\n\t" \
  "buffer_load_dwordx4 v255, %[srdV], s71 offen offset:3072 nt lds\n\t" \
  "v_max3_f32 v228, v228, v106, v107\n\t" \
  "v_mfma_f32_16x16x32_bf16 v[132:135], v[180:183], a[216:219], v[132:135]\n\t" \
  "s_add_u32 s95, s95, s62\n\t" \
  "s_sub_u32 s79, s95, 163840\n\t" \
  "s_cselect_b32 s95, s95, s79\n\t" \
  "v_max3_f32 v229, v229, v124, v125\n\t" \
  "v_max3_f32 v228, v228, v126, v127\n\t" \
  "v_mfma_f32_16x16x32_bf16 v[132:135], v[184:187], a[236:239], v[132:135]\n\t" \
  "s_add_u32 s65, s66, s62\n\t" \
  "s_sub_u32 s79, s65, 163840\n\t" \
  "s_cselect_b32 s65, s65, s79\n\t" \
  "v_mfma_f32_16x16x32_bf16 a[160:163], a[240:243], v[136:139], a[160:163]\n\t" \
  "s_waitcnt lgkmcnt(0)\n\t" \
  "v_max3_f32 v229, v229, v108, v109\n\t" \
  "v_mfma_f32_16x16x32_bf16 a[0:3], v[188:191], v[136:139], a[0:3]\n\t" \
  "v_max3_f32 v228, v228, v110, v111\n\t" \
  "v_max3_f32 v229, v229, v128, v129\n\t" \
  "v_mfma_f32_16x16x32_bf16 a[20:23], v[192:195], v[136:139], a[20:23]\n\t" \
  "s_mov_b32 m0, s95\n\t" \
  "s_xor_b32 s71, s67, s99\n\t" \
  "buffer_load_dwordx4 %[ko0], %[srdK], s71 offen nt lds\n\t" \
  "v_mfma_f32_16x16x32_bf16 a[40:43], v[196:199], v[136:139], a[40:43]\n\t" \
  "v_max3_f32 v228, v228, v130, v131\n\t" \
  "v_max3_f32 v229, v229, v112, v113\n\t" \
  "v_mfma_f32_16x16x32_bf16 a[60:63], v[200:203], v[136:139], a[60:63]\n\t" \
  "v_max3_f32 v228, v228, v114, v115\n\t" \
  "v_max3_f32 v229, v229, v132, v133\n\t" \
  "v_mfma_f32_16x16x32_bf16 a[80:83], v[204:207], v[136:139], a[80:83]\n\t" \
  "v_max3_f32 v228, v228, v134, v135\n\t" \
  "v_max_f32 v228, v228, v229\n\t" \
  "v_mfma_f32_16x16x32_bf16 a[100:103], v[208:211], v[136:139], a[100:103]\n\t" \
  "v_cmp_lt_f32 vcc, s74, v228\n\t" \
  "s_cbranch_vccnz .LtrAM_%=\n\t" \
  ".LbackAM_%=:\n\t" \
  "v_mfma_f32_16x16x32_bf16 a[120:123], v[212:215], v[136:139], a[120:123]\n\t" \
  "v_exp_f32 v96, v96\n\t" \
  "v_mfma_f32_16x16x32_bf16 a[140:143], v[216:219], v[136:139], a[140:143]\n\t" \
  "s_add_u32 s67, s67, 8192\n\t" \
  "buffer_load_dwordx4 v252, %[srdK], s71 offen offset:1024 nt lds\n\t" \
  "v_add_u32 v220, s65, %[kb]\n\t" \
  "v_mfma_f32_16x16x32_bf16 a[164:167], a[240:243], v[140:143], a[164:167]\n\t" \
  "v_exp_f32 v97, v97\n\t" \
  "v_mfma_f32_16x16x32_bf16 a[4:7], v[188:191], v[140:143], a[4:7]\n\t" \
  "v_exp_f32 v98, v98\n\t" \
  "v_mfma_f32_16x16x32_bf16 a[24:27], v[192:195], v[140:143], a[24:27]\n\t" \
  "v_exp_f32 v99, v99\n\t" \
  "v_mfma_f32_16x16x32_bf16 a[44:47], v[196:199], v[140:143], a[44:47]\n\t" \
  "buffer_load_dwordx4 v253, %[srdK], s71 offen offset:2048 nt lds\n\t" \
  "v_xor_b32 v221, 64, v220\n\t" \
  "v_mfma_f32_16x16x32_bf16 a[64:67], v[200:203], v[140:143], a[64:67]\n\t" \
  "v_exp_f32 v116, v116\n\t" \
  "v_mfma_f32_16x16x32_bf16 a[84:87], v[204:207], v[140:143], a[84:87]\n\t" \
  "v_exp_f32 v117, v117\n\t" \
  "v_mfma_f32_16x16x32_bf16 a[104:107], v[208:211], v[140:143], a[104:107]\n\t" \
  "v_exp_f32 v118, v118\n\t" \
  "v_mfma_f32_16x16x32_bf16 a[124:127], v[212:215], v[140:143], a[124:127]\n\t" \
  "buffer_load_dwordx4 v254, %[srdK], s71 offen offset:3072 nt lds\n\t" \
  "v_xor_b32 v222, 128, v220\n\t" \
  "v_mfma_f32_16x16x32_bf16 a[144:147], v[216:219], v[140:143], a[144:147]\n\t" \
  "v_exp_f32 v119, v119\n\t" \
  "v_mfma_f32_16x16x32_bf16 a[168:171], a[240:243], v[144:147], a[168:171]\n\t" \
  "v_exp_f32 v100, v100\n\t" \
  "v_mfma_f32_16x16x32_bf16 a[8:11], v[188:191], v[144:147], a[8:11]\n\t" \
  "v_exp_f32 v101, v101\n\t" \
  "v_mfma_f32_16x16x32_bf16 a[28:31], v[192:195], v[144:147], a[28:31]\n\t" \
  "s_add_u32 m0, s95, 4096\n\t" \
  "s_or_b32 s71, s71, 4096\n\t" \
  "buffer_load_dwordx4 %[ko0], %[srdK], s71 offen nt lds\n\t" \
  "v_xor_b32 v223, 192, v220\n\t" \
  "v_mfma_f32_16x16x32_bf16 a[48:51], v[196:199], v[144:147], a[48:51]\n\t" \
  "v_exp_f32 v102, v102\n\t" \
  "v_mfma_f32_16x16x32_bf16 a[68:71], v[200:203], v[144:147], a[68:71]\n\t" \
  "v_exp_f32 v103, v103\n\t" \
  "v_mfma_f32_16x16x32_bf16 a[88:91], v[204:207], v[144:147], a[88:91]\n\t" \
  "v_exp_f32 v120, v120\n\t" \
  "v_mfma_f32_16x16x32_bf16 a[108:111], v[208:211], v[144:147], a[108:111]\n\t" \
  "buffer_load_dwordx4 v252, %[srdK], s71 offen offset:1024 nt lds\n\t" \
  "v_cvt_pk_bf16_f32 v136, v96, v97\n\t" \
  "v_mfma_f32_16x16x32_bf16 a[128:131], v[212:215], v[144:147], a[128:131]\n\t" \
  "v_exp_f32 v121, v121\n\t" \
  "v_mfma_f32_16x16x32_bf16 a[148:151], v[216:219], v[144:147], a[148:151]\n\t" \
  "s_waitcnt vmcnt(14)\n\t" \
  "v_exp_f32 v122, v122\n\t" \
  "v_mfma_f32_16x16x32_bf16 a[172:175], a[240:243], v[148:151], a[172:175]\n\t" \
  "ds_read_b128 v[156:159], v220\n\t" \
  "v_cvt_pk_bf16_f32 v137, v98, v99\n\t" \
  "v_mfma_f32_16x16x32_bf16 a[12:15], v[188:191], v[148:151], a[12:15]\n\t" \
  "ds_read_b128 v[160:163], v221\n\t" \
  "v_cvt_pk_bf16_f32 v138, v116, v117\n\t" \
  "v_mfma_f32_16x16x32_bf16 a[32:35], v[192:195], v[148:151], a[32:35]\n\t" \
  "v_exp_f32 v123, v123\n\t" \
  "v_exp_f32 v104, v104\n\t" \
  "v_mfma_f32_16x16x32_bf16 a[52:55], v[196:199], v[148:151], a[52:55]\n\t" \
  "ds_read_b128 v[164:167], v222\n\t" \
  "v_cvt_pk_bf16_f32 v139, v118, v119\n\t" \
  "v_mfma_f32_16x16x32_bf16 a[72:75], v[200:203], v[148:151], a[72:75]\n\t" \
  "ds_read_b128 v[168:171], v223\n\t" \
  "v_cvt_pk_bf16_f32 v140, v100, v101\n\t" \
  "v_mfma_f32_16x16x32_bf16 a[92:95], v[204:207], v[148:151], a[92:95]\n\t" \
  "v_exp_f32 v105, v105\n\t" \
  "v_exp_f32 v106, v106\n\t" \
  "v_mfma_f32_16x16x32_bf16 a[112:115], v[208:211], v[148:151], a[112:115]\n\t" \
  "ds_read_b128 v[172:175], v220 offset:4096\n\t" \
  "v_cvt_pk_bf16_f32 v141, v102, v103\n\t" \
  "v_mfma_f32_16x16x32_bf16 a[132:135], v[212:215], v[148:151], a[132:135]\n\t" \
  "ds_read_b128 v[176:179], v221 offset:4096\n\t" \
  "v_cvt_pk_bf16_f32 v142, v120, v121\n\t" \
  "v_mfma_f32_16x16x32_bf16 a[152:155], v[216:219], v[148:151], a[152:155]\n\t" \
  "ds_read_b128 v[180:183], v222 offset:4096\n\t" \
  "v_cvt_pk_bf16_f32 v143, v122, v123\n\t" \
  "v_mfma_f32_16x16x32_bf16 a[176:179], a[240:243], v[152:155], a[176:179]\n\t" \
  "ds_read_b128 v[184:187], v223 offset:4096\n\t" \
  "v_mfma_f32_16x16x32_bf16 a[16:19], v[188:191], v[152:155], a[16:19]\n\t" \
  "v_exp_f32 v107, v107\n\t" \
  "v_exp_f32 v124, v124\n\t" \
  "v_mfma_f32_16x16x32_bf16 a[36:39], v[192:195], v[152:155], a[36:39]\n\t" \
  "buffer_load_dwordx4 v253, %[srdK], s71 offen offset:2048 nt lds\n\t" \
  "v_cvt_pk_bf16_f32 v144, v104, v105\n\t" \
  "v_mfma_f32_16x16x32_bf16 a[56:59], v[196:199], v[152:155], a[56:59]\n\t" \
  "v_exp_f32 v125, v125\n\t" \
  "v_exp_f32 v126, v126\n\t" \
  "v_mfma_f32_16x16x32_bf16 a[76:79], v[200:203], v[152:155], a[76:79]\n\t" \
  "v_exp_f32 v127, v127\n\t" \
  "v_mfma_f32_16x16x32_bf16 a[96:99], v[204:207], v[152:155], a[96:99]\n\t" \
  "buffer_load_dwordx4 v254, %[srdK], s71 offen offset:3072 nt lds\n\t" \
  "v_cvt_pk_bf16_f32 v145, v106, v107\n\t" \
  "v_mfma_f32_16x16x32_bf16 a[116:119], v[208:211], v[152:155], a[116:119]\n\t" \
  "s_add_u32 s95, s95, s62\n\t" \
  "s_sub_u32 s79, s95, 163840\n\t" \
  "s_cselect_b32 s95, s95, s79\n\t" \
  "v_cvt_pk_bf16_f32 v146, v124, v125\n\t" \
  "v_cvt_pk_bf16_f32 v147, v126, v127\n\t" \
  "v_mfma_f32_16x16x32_bf16 a[136:139], v[212:215], v[152:155], a[136:139]\n\t" \
  "s_add_u32 s66, s65, s62\n\t" \
  "s_sub_u32 s79, s66, 163840\n\t" \
  "s_cselect_b32 s66, s66, s79\n\t" \
  "v_mfma_f32_16x16x32_bf16 a[156:159], v[216:219], v[152:155], a[156:159]\n\t" \
  "s_cmp_lg_u32 s73, 0\n\t" \
  "s_cbranch_scc1 .LtrBM_%=\n\t" \
  ".LbackBM_%=:\n\t" \
  "s_cmp_lt_u32 s68, 122880\n\t" \
  "s_cbranch_scc1 .LloopM_%=\n\t" \
  ".Llast_%=:\n\t" \
  "s_waitcnt lgkmcnt(0)\n\t" \
  "s_cmp_lg_u32 s63, 0\n\t" \
  "s_cbranch_scc0 .Lce7_%=\n\t" \
  "s_waitcnt vmcnt(0)\n\t" \
  "s_barrier\n\t" \
  ".Lce7_%=:\n\t" \
  "v_mfma_f32_16x16x32_bf16 v[96:99], v[156:159], v[56:59], v[76:79]\n\t" \
  "s_cmp_lg_u32 s63, 0\n\t" \
  "s_cbranch_scc1 .Lce8_%=\n\t" \
  "s_swappc_b64 s[60:61], s[58:59]\n\t" \
  ".Lce8_%=:\n\t" \
  "v_add_u32 v252, 7168, %[wov]\n\t" \
  "v_mfma_f32_16x16x32_bf16 v[96:99], v[160:163], a[180:183], v[96:99]\n\t" \
  "v_add_u32 v253, 14336, %[wov]\n\t" \
  "v_exp_f32 v108, v108\n\t" \
  "v_mfma_f32_16x16x32_bf16 v[96:99], v[164:167], a[200:203], v[96:99]\n\t" \
  "v_add_u32 v254, 21504, %[wov]\n\t" \
  "v_exp_f32 v109, v109\n\t" \
  "v_mfma_f32_16x16x32_bf16 v[96:99], v[168:171], a[220:223], v[96:99]\n\t" \
  "v_add_u32 v220, s66, %[vb]\n\t" \
  "v_mfma_f32_16x16x32_bf16 v[116:119], v[172:175], v[56:59], v[76:79]\n\t" \
  "v_xor_b32 v221, 32, v220\n\t" \
  "v_exp_f32 v110, v110\n\t" \
  "v_mfma_f32_16x16x32_bf16 v[116:119], v[176:179], a[180:183], v[116:119]\n\t" \
  "v_xor_b32 v222, 64, v220\n\t" \
  "v_exp_f32 v111, v111\n\t" \
  "v_mfma_f32_16x16x32_bf16 v[116:119], v[180:183], a[200:203], v[116:119]\n\t" \
  "v_xor_b32 v223, 96, v220\n\t" \
  "v_mfma_f32_16x16x32_bf16 v[116:119], v[184:187], a[220:223], v[116:119]\n\t" \
  "v_xor_b32 v224, 128, v220\n\t" \
  "v_exp_f32 v128, v128\n\t" \
  "v_mfma_f32_16x16x32_bf16 v[100:103], v[156:159], v[60:63], v[80:83]\n\t" \
  "v_xor_b32 v225, 160, v220\n\t" \
  "v_exp_f32 v129, v129\n\t" \
  "v_mfma_f32_16x16x32_bf16 v[100:103], v[160:163], a[184:187], v[100:103]\n\t" \
  "v_xor_b32 v226, 192, v220\n\t" \
  "v_mfma_f32_16x16x32_bf16 v[100:103], v[164:167], a[204:207], v[100:103]\n\t" \
  "v_xor_b32 v227, 224, v220\n\t" \
  "v_exp_f32 v130, v130\n\t" \
  "v_mfma_f32_16x16x32_bf16 v[100:103], v[168:171], a[224:227], v[100:103]\n\t" \
  "s_waitcnt vmcnt(16)\n\t" \
  "v_exp_f32 v131, v131\n\t" \
  "v_mfma_f32_16x16x32_bf16 v[120:123], v[172:175], v[60:63], v[80:83]\n\t" \
  "ds_read_b64_tr_b16 v[188:189], v220\n\t" \
  "v_mfma_f32_16x16x32_bf16 v[120:123], v[176:179], a[184:187], v[120:123]\n\t" \
  "ds_read_b64_tr_b16 v[190:191], v220 offset:4096\n\t" \
  "v_exp_f32 v112, v112\n\t" \
  "v_mfma_f32_16x16x32_bf16 v[120:123], v[180:183], a[204:207], v[120:123]\n\t" \
  "ds_read_b64_tr_b16 v[192:193], v221\n\t" \
  "v_exp_f32 v113, v113\n\t" \
  "v_mfma_f32_16x16x32_bf16 v[120:123], v[184:187], a[224:227], v[120:123]\n\t" \
  "ds_read_b64_tr_b16 v[194:195], v221 offset:4096\n\t" \
  "v_mfma_f32_16x16x32_bf16 v[104:107], v[156:159], v[64:67], v[84:87]\n\t" \
  "ds_read_b64_tr_b16 v[196:197], v222\n\t" \
  "v_mfma_f32_16x16x32_bf16 v[104:107], v[160:163], a[188:191], v[104:107]\n\t" \
  "ds_read_b64_tr_b16 v[198:199], v222 offset:4096\n\t" \
  "v_exp_f32 v114, v114\n\t" \
  "v_mfma_f32_16x16x32_bf16 v[104:107], v[164:167], a[208:211], v[104:107]\n\t" \
  "ds_read_b64_tr_b16 v[200:201], v223\n\t" \
  "v_cvt_pk_bf16_f32 v148, v108, v109\n\t" \
  "v_mfma_f32_16x16x32_bf16 v[104:107], v[168:171], a[228:231], v[104:107]\n\t" \
  "ds_read_b64_tr_b16 v[202:203], v223 offset:4096\n\t" \
  "v_cvt_pk_bf16_f32 v149, v110, v111\n\t" \
  "v_mfma_f32_16x16x32_bf16 v[124:127], v[172:175], v[64:67], v[84:87]\n\t" \
  "ds_read_b64_tr_b16 v[204:205], v224\n\t" \
  "v_exp_f32 v115, v115\n\t" \
  "v_mfma_f32_16x16x32_bf16 v[124:127], v[176:179], a[188:191], v[124:127]\n\t" \
  "ds_read_b64_tr_b16 v[206:207], v224 offset:4096\n\t" \
  "v_cvt_pk_bf16_f32 v150, v128, v129\n\t" \
  "v_mfma_f32_16x16x32_bf16 v[124:127], v[180:183], a[208:211], v[124:127]\n\t" \
  "ds_read_b64_tr_b16 v[208:209], v225\n\t" \
  "v_cvt_pk_bf16_f32 v151, v130, v131\n\t" \
  "v_mfma_f32_16x16x32_bf16 v[124:127], v[184:187], a[228:231], v[124:127]\n\t" \
  "ds_read_b64_tr_b16 v[210:211], v225 offset:4096\n\t" \
  "v_exp_f32 v132, v132\n\t" \
  "v_mfma_f32_16x16x32_bf16 v[108:111], v[156:159], v[68:71], v[88:91]\n\t" \
  "ds_read_b64_tr_b16 v[212:213], v226\n\t" \
  "v_cvt_pk_bf16_f32 v152, v112, v113\n\t" \
  "v_mfma_f32_16x16x32_bf16 v[108:111], v[160:163], a[192:195], v[108:111]\n\t" \
  "ds_read_b64_tr_b16 v[214:215], v226 offset:4096\n\t" \
  "v_exp_f32 v133, v133\n\t" \
  "v_mfma_f32_16x16x32_bf16 v[108:111], v[164:167], a[212:215], v[108:111]\n\t" \
  "ds_read_b64_tr_b16 v[216:217], v227\n\t" \
  "v_cvt_pk_bf16_f32 v153, v114, v115\n\t" \
  "v_mfma_f32_16x16x32_bf16 v[108:111], v[168:171], a[232:235], v[108:111]\n\t" \
  "ds_read_b64_tr_b16 v[218:219], v227 offset:4096\n\t" \
  "v_exp_f32 v134, v134\n\t" \
  "v_mfma_f32_16x16x32_bf16 v[128:131], v[172:175], v[68:71], v[88:91]\n\t" \
  "s_add_u32 m0, s75, 0\n\t" \
  "s_mov_b32 s71, 0\n\t" \
  "buffer_load_dwordx4 %[wov], %[srdWo], s71 offen lds\n\t" \
  "v_exp_f32 v135, v135\n\t" \
  "v_mfma_f32_16x16x32_bf16 v[128:131], v[176:179], a[192:195], v[128:131]\n\t" \
  "buffer_load_dwordx4 v252, %[srdWo], s71 offen offset:1024 lds\n\t" \
  "v_cvt_pk_bf16_f32 v154, v132, v133\n\t" \
  "v_mfma_f32_16x16x32_bf16 v[128:131], v[180:183], a[212:215], v[128:131]\n\t" \
  "buffer_load_dwordx4 v253, %[srdWo], s71 offen offset:2048 lds\n\t" \
  "v_cvt_pk_bf16_f32 v155, v134, v135\n\t" \
  "v_mfma_f32_16x16x32_bf16 v[128:131], v[184:187], a[232:235], v[128:131]\n\t" \
  "buffer_load_dwordx4 v254, %[srdWo], s71 offen offset:3072 lds\n\t" \
  "v_max3_f32 v228, v96, v97, v98\n\t" \
  "v_mfma_f32_16x16x32_bf16 v[112:115], v[156:159], v[72:75], v[92:95]\n\t" \
  "v_max3_f32 v229, v99, v116, v117\n\t" \
  "v_mfma_f32_16x16x32_bf16 v[112:115], v[160:163], a[196:199], v[112:115]\n\t" \
  "s_add_u32 m0, s75, 4096\n\t" \
  "s_mov_b32 s71, 32768\n\t" \
  "buffer_load_dwordx4 %[wov], %[srdWo], s71 offen lds\n\t" \
  "v_max3_f32 v228, v228, v118, v119\n\t" \
  "v_mfma_f32_16x16x32_bf16 v[112:115], v[164:167], a[216:219], v[112:115]\n\t" \
  "buffer_load_dwordx4 v252, %[srdWo], s71 offen offset:1024 lds\n\t" \
  "v_max3_f32 v229, v229, v100, v101\n\t" \
  "v_mfma_f32_16x16x32_bf16 v[112:115], v[168:171], a[236:239], v[112:115]\n\t" \
  "buffer_load_dwordx4 v253, %[srdWo], s71 offen offset:2048 lds\n\t" \
  "v_max3_f32 v228, v228, v102, v103\n\t" \
  "v_max3_f32 v229, v229, v120, v121\n\t" \
  "v_mfma_f32_16x16x32_bf16 v[132:135], v[172:175], v[72:75], v[92:95]\n\t" \
  "buffer_load_dwordx4 v254, %[srdWo], s71 offen offset:3072 lds\n\t" \
  "v_max3_f32 v228, v228, v122, v123\n\t" \
  "v_max3_f32 v229, v229, v104, v105\n\t" \
  "v_mfma_f32_16x16x32_bf16 v[132:135], v[176:179], a[196:199], v[132:135]\n\t" \
  "v_max3_f32 v228, v228, v106, v107\n\t" \
  "v_mfma_f32_16x16x32_bf16 v[132:135], v[180:183], a[216:219], v[132:135]\n\t" \
  "v_max3_f32 v229, v229, v124, v125\n\t" \
  "v_max3_f32 v228, v228, v126, v127\n\t" \
  "v_mfma_f32_16x16x32_bf16 v[132:135], v[184:187], a[236:239], v[132:135]\n\t" \
  "s_add_u32 s65, s66, s62\n\t" \
  "s_sub_u32 s79, s65, 163840\n\t" \
  "s_cselect_b32 s65, s65, s79\n\t" \
  "s_add_u32 s66, s65, s62\n\t" \
  "s_sub_u32 s79, s66, 163840\n\t" \
  "s_cselect_b32 s66, s66, s79\n\t" \
  "v_mfma_f32_16x16x32_bf16 a[160:163], a[240:243], v[136:139], a[160:163]\n\t" \
  "s_waitcnt lgkmcnt(0)\n\t" \
  "v_max3_f32 v229, v229, v108, v109\n\t" \
  "v_mfma_f32_16x16x32_bf16 a[0:3], v[188:191], v[136:139], a[0:3]\n\t" \
  "v_max3_f32 v228, v228, v110, v111\n\t" \
  "v_max3_f32 v229, v229, v128, v129\n\t" \
  "v_mfma_f32_16x16x32_bf16 a[20:23], v[192:195], v[136:139], a[20:23]\n\t" \
  "s_add_u32 m0, s76, 0\n\t" \
  "s_mov_b32 s71, 65536\n\t" \
  "buffer_load_dwordx4 %[wov], %[srdWo], s71 offen lds\n\t" \
  "v_mfma_f32_16x16x32_bf16 a[40:43], v[196:199], v[136:139], a[40:43]\n\t" \
  "v_max3_f32 v228, v228, v130, v131\n\t" \
  "v_max3_f32 v229, v229, v112, v113\n\t" \
  "v_mfma_f32_16x16x32_bf16 a[60:63], v[200:203], v[136:139], a[60:63]\n\t" \
  "buffer_load_dwordx4 v252, %[srdWo], s71 offen offset:1024 lds\n\t" \
  "v_max3_f32 v228, v228, v114, v115\n\t" \
  "v_max3_f32 v229, v229, v132, v133\n\t" \
  "v_mfma_f32_16x16x32_bf16 a[80:83], v[204:207], v[136:139], a[80:83]\n\t" \
  "v_max3_f32 v228, v228, v134, v135\n\t" \
  "v_max_f32 v228, v228, v229\n\t" \
  "v_mfma_f32_16x16x32_bf16 a[100:103], v[208:211], v[136:139], a[100:103]\n\t" \
  "v_cmp_lt_f32 vcc, s74, v228\n\t" \
  "s_cbranch_vccnz .LtrAL_%=\n\t" \
  ".LbackAL_%=:\n\t" \
  "v_mfma_f32_16x16x32_bf16 a[120:123], v[212:215], v[136:139], a[120:123]\n\t" \
  "buffer_load_dwordx4 v253, %[srdWo], s71 offen offset:2048 lds\n\t" \
  "v_exp_f32 v96, v96\n\t" \
  "v_mfma_f32_16x16x32_bf16 a[140:143], v[216:219], v[136:139], a[140:143]\n\t" \
  "v_mfma_f32_16x16x32_bf16 a[164:167], a[240:243], v[140:143], a[164:167]\n\t" \
  "buffer_load_dwordx4 v254, %[srdWo], s71 offen offset:3072 lds\n\t" \
  "v_exp_f32 v97, v97\n\t" \
  "v_mfma_f32_16x16x32_bf16 a[4:7], v[188:191], v[140:143], a[4:7]\n\t" \
  "v_exp_f32 v98, v98\n\t" \
  "v_mfma_f32_16x16x32_bf16 a[24:27], v[192:195], v[140:143], a[24:27]\n\t" \
  "v_exp_f32 v99, v99\n\t" \
  "v_mfma_f32_16x16x32_bf16 a[44:47], v[196:199], v[140:143], a[44:47]\n\t" \
  "s_add_u32 m0, s76, 4096\n\t" \
  "s_mov_b32 s71, 98304\n\t" \
  "buffer_load_dwordx4 %[wov], %[srdWo], s71 offen lds\n\t" \
  "v_mfma_f32_16x16x32_bf16 a[64:67], v[200:203], v[140:143], a[64:67]\n\t" \
  "v_exp_f32 v116, v116\n\t" \
  "v_mfma_f32_16x16x32_bf16 a[84:87], v[204:207], v[140:143], a[84:87]\n\t" \
  "buffer_load_dwordx4 v252, %[srdWo], s71 offen offset:1024 lds\n\t" \
  "v_exp_f32 v117, v117\n\t" \
  "v_mfma_f32_16x16x32_bf16 a[104:107], v[208:211], v[140:143], a[104:107]\n\t" \
  "v_exp_f32 v118, v118\n\t" \
  "v_mfma_f32_16x16x32_bf16 a[124:127], v[212:215], v[140:143], a[124:127]\n\t" \
  "v_add_u32 v220, s66, %[vb]\n\t" \
  "v_mfma_f32_16x16x32_bf16 a[144:147], v[216:219], v[140:143], a[144:147]\n\t" \
  "buffer_load_dwordx4 v253, %[srdWo], s71 offen offset:2048 lds\n\t" \
  "v_xor_b32 v221, 32, v220\n\t" \
  "v_exp_f32 v119, v119\n\t" \
  "v_mfma_f32_16x16x32_bf16 a[168:171], a[240:243], v[144:147], a[168:171]\n\t" \
  "v_xor_b32 v222, 64, v220\n\t" \
  "v_exp_f32 v100, v100\n\t" \
  "v_mfma_f32_16x16x32_bf16 a[8:11], v[188:191], v[144:147], a[8:11]\n\t" \
  "buffer_load_dwordx4 v254, %[srdWo], s71 offen offset:3072 lds\n\t" \
  "v_xor_b32 v223, 96, v220\n\t" \
  "v_exp_f32 v101, v101\n\t" \
  "v_mfma_f32_16x16x32_bf16 a[28:31], v[192:195], v[144:147], a[28:31]\n\t" \
  "v_xor_b32 v224, 128, v220\n\t" \
  "v_mfma_f32_16x16x32_bf16 a[48:51], v[196:199], v[144:147], a[48:51]\n\t" \
  "v_xor_b32 v225, 160, v220\n\t" \
  "v_exp_f32 v102, v102\n\t" \
  "v_mfma_f32_16x16x32_bf16 a[68:71], v[200:203], v[144:147], a[68:71]\n\t" \
  "v_xor_b32 v226, 192, v220\n\t" \
  "v_exp_f32 v103, v103\n\t" \
  "v_mfma_f32_16x16x32_bf16 a[88:91], v[204:207], v[144:147], a[88:91]\n\t" \
  "v_xor_b32 v227, 224, v220\n\t" \
  "v_exp_f32 v120, v120\n\t" \
  "v_mfma_f32_16x16x32_bf16 a[108:111], v[208:211], v[144:147], a[108:111]\n\t" \
  "v_cvt_pk_bf16_f32 v136, v96, v97\n\t" \
  "v_mfma_f32_16x16x32_bf16 a[128:131], v[212:215], v[144:147], a[128:131]\n\t" \
  "s_waitcnt vmcnt(16)\n\t" \
  "v_exp_f32 v121, v121\n\t" \
  "v_mfma_f32_16x16x32_bf16 a[148:151], v[216:219], v[144:147], a[148:151]\n\t" \
  "ds_read_b64_tr_b16 v[156:157], v220\n\t" \
  "v_exp_f32 v122, v122\n\t" \
  "v_mfma_f32_16x16x32_bf16 a[172:175], a[240:243], v[148:151], a[172:175]\n\t" \
  "ds_read_b64_tr_b16 v[158:159], v220 offset:4096\n\t" \
  "v_cvt_pk_bf16_f32 v137, v98, v99\n\t" \
  "v_mfma_f32_16x16x32_bf16 a[12:15], v[188:191], v[148:151], a[12:15]\n\t" \
  "ds_read_b64_tr_b16 v[160:161], v221\n\t" \
  "v_cvt_pk_bf16_f32 v138, v116, v117\n\t" \
  "v_mfma_f32_16x16x32_bf16 a[32:35], v[192:195], v[148:151], a[32:35]\n\t" \
  "ds_read_b64_tr_b16 v[162:163], v221 offset:4096\n\t" \
  "v_exp_f32 v123, v123\n\t" \
  "v_exp_f32 v104, v104\n\t" \
  "v_mfma_f32_16x16x32_bf16 a[52:55], v[196:199], v[148:151], a[52:55]\n\t" \
  "ds_read_b64_tr_b16 v[164:165], v222\n\t" \
  "v_cvt_pk_bf16_f32 v139, v118, v119\n\t" \
  "v_mfma_f32_16x16x32_bf16 a[72:75], v[200:203], v[148:151], a[72:75]\n\t" \
  "ds_read_b64_tr_b16 v[166:167], v222 offset:4096\n\t" \
  "v_cvt_pk_bf16_f32 v140, v100, v101\n\t" \
  "v_mfma_f32_16x16x32_bf16 a[92:95], v[204:207], v[148:151], a[92:95]\n\t" \
  "ds_read_b64_tr_b16 v[168:169], v223\n\t" \
  "v_exp_f32 v105, v105\n\t" \
  "v_exp_f32 v106, v106\n\t" \
  "v_mfma_f32_16x16x32_bf16 a[112:115], v[208:211], v[148:151], a[112:115]\n\t" \
  "ds_read_b64_tr_b16 v[170:171], v223 offset:4096\n\t" \
  "v_cvt_pk_bf16_f32 v141, v102, v103\n\t" \
  "v_mfma_f32_16x16x32_bf16 a[132:135], v[212:215], v[148:151], a[132:135]\n\t" \
  "ds_read_b64_tr_b16 v[172:173], v224\n\t" \
  "v_cvt_pk_bf16_f32 v142, v120, v121\n\t" \
  "v_mfma_f32_16x16x32_bf16 a[152:155], v[216:219], v[148:151], a[152:155]\n\t" \
  "ds_read_b64_tr_b16 v[174:175], v224 offset:4096\n\t" \
  "v_cvt_pk_bf16_f32 v143, v122, v123\n\t" \
  "v_mfma_f32_16x16x32_bf16 a[176:179], a[240:243], v[152:155], a[176:179]\n\t" \
  "ds_read_b64_tr_b16 v[176:177], v225\n\t" \
  "v_mfma_f32_16x16x32_bf16 a[16:19], v[188:191], v[152:155], a[16:19]\n\t" \
  "ds_read_b64_tr_b16 v[178:179], v225 offset:4096\n\t" \
  "v_exp_f32 v107, v107\n\t" \
  "v_exp_f32 v124, v124\n\t" \
  "v_mfma_f32_16x16x32_bf16 a[36:39], v[192:195], v[152:155], a[36:39]\n\t" \
  "ds_read_b64_tr_b16 v[180:181], v226\n\t" \
  "v_cvt_pk_bf16_f32 v144, v104, v105\n\t" \
  "v_mfma_f32_16x16x32_bf16 a[56:59], v[196:199], v[152:155], a[56:59]\n\t" \
  "ds_read_b64_tr_b16 v[182:183], v226 offset:4096\n\t" \
  "v_exp_f32 v125, v125\n\t" \
  "v_exp_f32 v126, v126\n\t" \
  "v_mfma_f32_16x16x32_bf16 a[76:79], v[200:203], v[152:155], a[76:79]\n\t" \
  "ds_read_b64_tr_b16 v[184:185], v227\n\t" \
  "v_exp_f32 v127, v127\n\t" \
  "v_mfma_f32_16x16x32_bf16 a[96:99], v[204:207], v[152:155], a[96:99]\n\t" \
  "ds_read_b64_tr_b16 v[186:187], v227 offset:4096\n\t" \
  "v_cvt_pk_bf16_f32 v145, v106, v107\n\t" \
  "v_mfma_f32_16x16x32_bf16 a[116:119], v[208:211], v[152:155], a[116:119]\n\t" \
  "v_cvt_pk_bf16_f32 v146, v124, v125\n\t" \
  "v_cvt_pk_bf16_f32 v147, v126, v127\n\t" \
  "v_mfma_f32_16x16x32_bf16 a[136:139], v[212:215], v[152:155], a[136:139]\n\t" \
  "v_mfma_f32_16x16x32_bf16 a[156:159], v[216:219], v[152:155], a[156:159]\n\t" \
  "s_cmp_lg_u32 s73, 0\n\t" \
  "s_cbranch_scc1 .LtrBL_%=\n\t" \
  ".LbackBL_%=:\n\t" \
  "v_add_u32 v233, s77, %[l16]\n\t" \
  "v_add_u32 v234, s80, %[l16]\n\t" \
  "v_add_u32 v235, s81, %[l16]\n\t" \
  "s_waitcnt lgkmcnt(0)\n\t" \
  "s_cmp_lg_u32 s63, 0\n\t" \
  "s_cbranch_scc0 .Lce9_%=\n\t" \
  "s_barrier\n\t" \
  ".Lce9_%=:\n\t" \
  "v_mfma_f32_16x16x32_bf16 a[80:83], v[172:175], v[136:139], a[80:83]\n\t" \
  "v_exp_f32 v108, v108\n\t" \
  "v_mfma_f32_16x16x32_bf16 a[100:103], v[176:179], v[136:139], a[100:103]\n\t" \
  "v_exp_f32 v109, v109\n\t" \
  "v_mfma_f32_16x16x32_bf16 a[120:123], v[180:183], v[136:139], a[120:123]\n\t" \
  "v_exp_f32 v110, v110\n\t" \
  "v_xor_b32 v236, 0x2000, v233\n\t" \
  "v_mfma_f32_16x16x32_bf16 a[140:143], v[184:187], v[136:139], a[140:143]\n\t" \
  "v_exp_f32 v111, v111\n\t" \
  "v_xor_b32 v237, 0x2000, v234\n\t" \
  "v_mfma_f32_16x16x32_bf16 a[0:3], v[156:159], v[136:139], a[0:3]\n\t" \
  "v_exp_f32 v128, v128\n\t" \
  "v_xor_b32 v238, 0x2000, v235\n\t" \
  "v_mfma_f32_16x16x32_bf16 a[20:23], v[160:163], v[136:139], a[20:23]\n\t" \
  "v_exp_f32 v129, v129\n\t" \
  "v_lshrrev_b32 v220, 1, %[l16]\n\t" \
  "v_mfma_f32_16x16x32_bf16 a[40:43], v[164:167], v[136:139], a[40:43]\n\t" \
  "v_exp_f32 v130, v130\n\t" \
  "v_sub_u32 v220, v235, v220\n\t" \
  "v_mfma_f32_16x16x32_bf16 a[60:63], v[168:171], v[136:139], a[60:63]\n\t" \
  "v_exp_f32 v131, v131\n\t" \
  "v_xor_b32 v221, 0x2000, v220\n\t" \
  "v_mfma_f32_16x16x32_bf16 a[160:163], a[240:243], v[136:139], a[160:163]\n\t" \
  "v_cvt_pk_bf16_f32 v148, v108, v109\n\t" \
  "v_cvt_pk_bf16_f32 v149, v110, v111\n\t" \
  "v_exp_f32 v112, v112\n\t" \
  "v_mfma_f32_16x16x32_bf16 a[84:87], v[172:175], v[140:143], a[84:87]\n\t" \
  "v_cvt_pk_bf16_f32 v150, v128, v129\n\t" \
  "v_cvt_pk_bf16_f32 v151, v130, v131\n\t" \
  "v_exp_f32 v113, v113\n\t" \
  "v_mfma_f32_16x16x32_bf16 a[104:107], v[176:179], v[140:143], a[104:107]\n\t" \
  "v_exp_f32 v114, v114\n\t" \
  "v_mfma_f32_16x16x32_bf16 a[124:127], v[180:183], v[140:143], a[124:127]\n\t" \
  "v_exp_f32 v115, v115\n\t" \
  "v_mfma_f32_16x16x32_bf16 a[144:147], v[184:187], v[140:143], a[144:147]\n\t" \
  "v_exp_f32 v132, v132\n\t" \
  "v_mfma_f32_16x16x32_bf16 a[4:7], v[156:159], v[140:143], a[4:7]\n\t" \
  "ds_write_b128 v233, a[80:83] offset:0\n\t" \
  "v_exp_f32 v133, v133\n\t" \
  "v_accvgpr_read_b32 v56, a0\n\t" \
  "v_accvgpr_read_b32 v57, a1\n\t" \
  "v_mfma_f32_16x16x32_bf16 a[24:27], v[160:163], v[140:143], a[24:27]\n\t" \
  "v_exp_f32 v134, v134\n\t" \
  "v_accvgpr_read_b32 v58, a2\n\t" \
  "v_accvgpr_read_b32 v59, a3\n\t" \
  "v_mfma_f32_16x16x32_bf16 a[44:47], v[164:167], v[140:143], a[44:47]\n\t" \
  "ds_write_b128 v233, a[100:103] offset:1024\n\t" \
  "v_exp_f32 v135, v135\n\t" \
  "v_accvgpr_read_b32 v60, a20\n\t" \
  "v_accvgpr_read_b32 v61, a21\n\t" \
  "v_mfma_f32_16x16x32_bf16 a[64:67], v[168:171], v[140:143], a[64:67]\n\t" \
  "v_cvt_pk_bf16_f32 v152, v112, v113\n\t" \
  "v_cvt_pk_bf16_f32 v153, v114, v115\n\t" \
  "v_accvgpr_read_b32 v62, a22\n\t" \
  "v_accvgpr_read_b32 v63, a23\n\t" \
  "v_mfma_f32_16x16x32_bf16 a[164:167], a[240:243], v[140:143], a[164:167]\n\t" \
  "ds_write_b128 v234, a[120:123] offset:2048\n\t" \
  "v_cvt_pk_bf16_f32 v154, v132, v133\n\t" \
  "v_cvt_pk_bf16_f32 v155, v134, v135\n\t" \
  "v_accvgpr_read_b32 v64, a40\n\t" \
  "v_accvgpr_read_b32 v65, a41\n\t" \
  "v_mfma_f32_16x16x32_bf16 a[88:91], v[172:175], v[144:147], a[88:91]\n\t" \
  "v_accvgpr_read_b32 v66, a42\n\t" \
  "v_accvgpr_read_b32 v67, a43\n\t" \
  "v_mfma_f32_16x16x32_bf16 a[108:111], v[176:179], v[144:147], a[108:111]\n\t" \
  "ds_write_b128 v234, a[140:143] offset:3072\n\t" \
  "v_accvgpr_read_b32 v68, a60\n\t" \
  "v_accvgpr_read_b32 v69, a61\n\t" \
  "v_mfma_f32_16x16x32_bf16 a[128:131], v[180:183], v[144:147], a[128:131]\n\t" \
  "v_accvgpr_read_b32 v70, a62\n\t" \
  "v_accvgpr_read_b32 v71, a63\n\t" \
  "v_mfma_f32_16x16x32_bf16 a[148:151], v[184:187], v[144:147], a[148:151]\n\t" \
  "v_accvgpr_read_b32 v77, a160\n\t" \
  "v_mfma_f32_16x16x32_bf16 a[8:11], v[156:159], v[144:147], a[8:11]\n\t" \
  "ds_write_b64 v220, v[76:77] offset:4096\n\t" \
  "v_accvgpr_read_b32 v188, a4\n\t" \
  "v_accvgpr_read_b32 v189, a5\n\t" \
  "v_mfma_f32_16x16x32_bf16 a[28:31], v[160:163], v[144:147], a[28:31]\n\t" \
  "ds_write_b128 v233, a[84:87] offset:2048\n\t" \
  "v_accvgpr_read_b32 v190, a6\n\t" \
  "v_accvgpr_read_b32 v191, a7\n\t" \
  "v_mfma_f32_16x16x32_bf16 a[48:51], v[164:167], v[144:147], a[48:51]\n\t" \
  "ds_write_b128 v233, a[104:107] offset:3072\n\t" \
  "v_accvgpr_read_b32 v192, a24\n\t" \
  "v_accvgpr_read_b32 v193, a25\n\t" \
  "v_mfma_f32_16x16x32_bf16 a[68:71], v[168:171], v[144:147], a[68:71]\n\t" \
  "v_accvgpr_read_b32 v194, a26\n\t" \
  "v_accvgpr_read_b32 v195, a27\n\t" \
  "v_mfma_f32_16x16x32_bf16 a[168:171], a[240:243], v[144:147], a[168:171]\n\t" \
  "ds_write_b128 v234, a[124:127] offset:4096\n\t" \
  "v_accvgpr_read_b32 v196, a44\n\t" \
  "v_accvgpr_read_b32 v197, a45\n\t" \
  "v_mfma_f32_16x16x32_bf16 a[92:95], v[172:175], v[148:151], a[92:95]\n\t" \
  "v_accvgpr_read_b32 v198, a46\n\t" \
  "v_accvgpr_read_b32 v199, a47\n\t" \
  "v_mfma_f32_16x16x32_bf16 a[112:115], v[176:179], v[148:151], a[112:115]\n\t" \
  "ds_write_b128 v234, a[144:147] offset:5120\n\t" \
  "v_accvgpr_read_b32 v200, a64\n\t" \
  "v_accvgpr_read_b32 v201, a65\n\t" \
  "v_mfma_f32_16x16x32_bf16 a[132:135], v[180:183], v[148:151], a[132:135]\n\t" \
  "v_accvgpr_read_b32 v202, a66\n\t" \
  "v_accvgpr_read_b32 v203, a67\n\t" \
  "v_mfma_f32_16x16x32_bf16 a[152:155], v[184:187], v[148:151], a[152:155]\n\t" \
  "v_accvgpr_read_b32 v81, a164\n\t" \
  "v_mfma_f32_16x16x32_bf16 a[12:15], v[156:159], v[148:151], a[12:15]\n\t" \
  "ds_write_b64 v220, v[80:81] offset:4608\n\t" \
  "v_accvgpr_read_b32 v204, a8\n\t" \
  "v_accvgpr_read_b32 v205, a9\n\t" \
  "v_mfma_f32_16x16x32_bf16 a[32:35], v[160:163], v[148:151], a[32:35]\n\t" \
  "ds_write_b128 v233, a[88:91] offset:4096\n\t" \
  "v_accvgpr_read_b32 v206, a10\n\t" \
  "v_accvgpr_read_b32 v207, a11\n\t" \
  "v_mfma_f32_16x16x32_bf16 a[52:55], v[164:167], v[148:151], a[52:55]\n\t" \
  "ds_write_b128 v233, a[108:111] offset:5120\n\t" \
  "v_accvgpr_read_b32 v208, a28\n\t" \
  "v_accvgpr_read_b32 v209, a29\n\t" \
  "v_mfma_f32_16x16x32_bf16 a[72:75], v[168:171], v[148:151], a[72:75]\n\t" \
  "v_accvgpr_read_b32 v210, a30\n\t" \
  "v_accvgpr_read_b32 v211, a31\n\t" \
  "v_mfma_f32_16x16x32_bf16 a[172:175], a[240:243], v[148:151], a[172:175]\n\t" \
  "ds_write_b128 v234, a[128:131] offset:6144\n\t" \
  "v_accvgpr_read_b32 v212, a48\n\t" \
  "v_accvgpr_read_b32 v213, a49\n\t" \
  "v_mfma_f32_16x16x32_bf16 a[96:99], v[172:175], v[152:155], a[96:99]\n\t" \
  "v_accvgpr_read_b32 v214, a50\n\t" \
  "v_accvgpr_read_b32 v215, a51\n\t" \
  "v_mfma_f32_16x16x32_bf16 a[116:119], v[176:179], v[152:155], a[116:119]\n\t" \
  "ds_write_b128 v234, a[148:151] offset:7168\n\t" \
  "v_accvgpr_read_b32 v216, a68\n\t" \
  "v_accvgpr_read_b32 v217, a69\n\t" \
  "v_mfma_f32_16x16x32_bf16 a[136:139], v[180:183], v[152:155], a[136:139]\n\t" \
  "v_accvgpr_read_b32 v218, a70\n\t" \
  "v_accvgpr_read_b32 v219, a71\n\t" \
  "v_mfma_f32_16x16x32_bf16 a[156:159], v[184:187], v[152:155], a[156:159]\n\t" \
  "v_accvgpr_read_b32 v85, a168\n\t" \
  "v_mfma_f32_16x16x32_bf16 a[16:19], v[156:159], v[152:155], a[16:19]\n\t" \
  "ds_write_b64 v220, v[84:85] offset:5120\n\t" \
  "v_accvgpr_read_b32 v96, a12\n\t" \
  "v_accvgpr_read_b32 v97, a13\n\t" \
  "v_accvgpr_read_b32 v98, a14\n\t" \
  "v_accvgpr_read_b32 v99, a15\n\t" \
  "v_mfma_f32_16x16x32_bf16 a[36:39], v[160:163], v[152:155], a[36:39]\n\t" \
  "ds_write_b128 v233, a[92:95] offset:6144\n\t" \
  "v_accvgpr_read_b32 v100, a32\n\t" \
  "v_accvgpr_read_b32 v101, a33\n\t" \
  "v_accvgpr_read_b32 v102, a34\n\t" \
  "v_mfma_f32_16x16x32_bf16 a[56:59], v[164:167], v[152:155], a[56:59]\n\t" \
  "ds_write_b128 v233, a[112:115] offset:7168\n\t" \
  "v_accvgpr_read_b32 v103, a35\n\t" \
  "v_accvgpr_read_b32 v104, a52\n\t" \
  "v_accvgpr_read_b32 v105, a53\n\t" \
  "v_accvgpr_read_b32 v106, a54\n\t" \
  "v_mfma_f32_16x16x32_bf16 a[76:79], v[168:171], v[152:155], a[76:79]\n\t" \
  "ds_write_b128 v235, a[132:135] offset:0\n\t" \
  "v_accvgpr_read_b32 v107, a55\n\t" \
  "v_accvgpr_read_b32 v108, a72\n\t" \
  "v_accvgpr_read_b32 v109, a73\n\t" \
  "v_mfma_f32_16x16x32_bf16 a[176:179], a[240:243], v[152:155], a[176:179]\n\t" \
  "ds_write_b128 v235, a[152:155] offset:1024\n\t" \
  "v_accvgpr_read_b32 v110, a74\n\t" \
  "v_accvgpr_read_b32 v111, a75\n\t" \
  "v_accvgpr_read_b32 v89, a172\n\t" \
  "ds_write_b64 v220, v[88:89] offset:5632\n\t" \
  "ds_write_b128 v234, a[96:99] offset:0\n\t" \
  "ds_write_b128 v234, a[116:119] offset:1024\n\t" \
  "ds_write_b128 v235, a[136:139] offset:2048\n\t" \
  "ds_write_b128 v235, a[156:159] offset:3072\n\t" \
  "s_nop 3\n\t" \
  "v_accvgpr_read_b32 v93, a176\n\t" \
  "ds_write_b64 v220, v[92:93] offset:6144\n\t" \
  "s_waitcnt lgkmcnt(0)\n\t" \
  "s_barrier\n\t" \
  "ds_read_b64 v[78:79], v221 offset:4096\n\t" \
  "ds_read_b64 v[82:83], v221 offset:4608\n\t" \
  "ds_read_b64 v[86:87], v221 offset:5120\n\t" \
  "ds_read_b64 v[90:91], v221 offset:5632\n\t" \
  "ds_read_b64 v[94:95], v221 offset:6144\n\t" \
  "ds_read_b128 v[136:139], v236 offset:0\n\t" \
  "ds_read_b128 v[140:143], v236 offset:1024\n\t" \
  "ds_read_b128 v[144:147], v237 offset:2048\n\t" \
  "ds_read_b128 v[148:151], v237 offset:3072\n\t" \
  "ds_read_b128 v[152:155], v236 offset:2048\n\t" \
  "ds_read_b128 v[156:159], v236 offset:3072\n\t" \
  "ds_read_b128 v[160:163], v237 offset:4096\n\t" \
  "ds_read_b128 v[164:167], v237 offset:5120\n\t" \
  "v_accvgpr_read_b32 v112, a16\n\t" \
  "v_accvgpr_read_b32 v113, a17\n\t" \
  "v_accvgpr_read_b32 v114, a18\n\t" \
  "v_accvgpr_read_b32 v115, a19\n\t" \
  "v_accvgpr_read_b32 v116, a36\n\t" \
  "v_accvgpr_read_b32 v117, a37\n\t" \
  "v_accvgpr_read_b32 v118, a38\n\t" \
  "v_accvgpr_read_b32 v119, a39\n\t" \
  "v_accvgpr_read_b32 v120, a56\n\t" \
  "v_accvgpr_read_b32 v121, a57\n\t" \
  "v_accvgpr_read_b32 v122, a58\n\t" \
  "v_accvgpr_read_b32 v123, a59\n\t" \
  "v_accvgpr_read_b32 v124, a76\n\t" \
  "v_accvgpr_read_b32 v125, a77\n\t" \
  "v_accvgpr_read_b32 v126, a78\n\t" \
  "v_accvgpr_read_b32 v127, a79\n\t" \
  "s_waitcnt lgkmcnt(8)\n\t" \
  "ds_read_b128 v[168:171], v236 offset:4096\n\t" \
  "ds_read_b128 v[172:175], v236 offset:5120\n\t" \
  "ds_read_b128 v[176:179], v237 offset:6144\n\t" \
  "ds_read_b128 v[180:183], v237 offset:7168\n\t" \
  "v_min_f32 v228, v76, v78\n\t" \
  "v_sub_f32 v76, v228, v76\n\t" \
  "v_sub_f32 v78, v228, v78\n\t" \
  "v_exp_f32 v76, v76\n\t" \
  "v_exp_f32 v78, v78\n\t" \
  "v_min_f32 v229, v80, v82\n\t" \
  "v_sub_f32 v80, v229, v80\n\t" \
  "v_sub_f32 v82, v229, v82\n\t" \
  "v_exp_f32 v80, v80\n\t" \
  "v_exp_f32 v82, v82\n\t" \
  "v_min_f32 v230, v84, v86\n\t" \
  "v_sub_f32 v84, v230, v84\n\t" \
  "v_sub_f32 v86, v230, v86\n\t" \
  "v_exp_f32 v84, v84\n\t" \
  "v_exp_f32 v86, v86\n\t" \
  "v_min_f32 v231, v88, v90\n\t" \
  "v_sub_f32 v88, v231, v88\n\t" \
  "v_sub_f32 v90, v231, v90\n\t" \
  "v_exp_f32 v88, v88\n\t" \
  "v_exp_f32 v90, v90\n\t" \
  "v_min_f32 v232, v92, v94\n\t" \
  "v_sub_f32 v92, v232, v92\n\t" \
  "v_sub_f32 v94, v232, v94\n\t" \
  "v_exp_f32 v92, v92\n\t" \
  "v_exp_f32 v94, v94\n\t" \
  "v_mul_f32 v77, v77, v76\n\t" \
  "v_fmac_f32 v77, v79, v78\n\t" \
  "v_rcp_f32 v79, v77\n\t" \
  "v_mul_f32 v81, v81, v80\n\t" \
  "v_fmac_f32 v81, v83, v82\n\t" \
  "v_rcp_f32 v83, v81\n\t" \
  "v_mul_f32 v85, v85, v84\n\t" \
  "v_fmac_f32 v85, v87, v86\n\t" \
  "v_rcp_f32 v87, v85\n\t" \
  "v_mul_f32 v89, v89, v88\n\t" \
  "v_fmac_f32 v89, v91, v90\n\t" \
  "v_rcp_f32 v91, v89\n\t" \
  "v_mul_f32 v93, v93, v92\n\t" \
  "v_fmac_f32 v93, v95, v94\n\t" \
  "v_rcp_f32 v95, v93\n\t" \
  "s_nop 0\n\t" \
  "v_mul_f32 v76, v76, v79\n\t" \
  "v_mul_f32 v77, v78, v79\n\t" \
  "v_mul_f32 v80, v80, v83\n\t" \
  "v_mul_f32 v81, v82, v83\n\t" \
  "v_mul_f32 v84, v84, v87\n\t" \
  "v_mul_f32 v85, v86, v87\n\t" \
  "v_mul_f32 v88, v88, v91\n\t" \
  "v_mul_f32 v89, v90, v91\n\t" \
  "v_mul_f32 v92, v92, v95\n\t" \
  "v_mul_f32 v93, v94, v95\n\t" \
  "s_waitcnt lgkmcnt(8)\n\t" \
  "v_pk_mul_f32 v[56:57], v[56:57], v[76:77] op_sel_hi:[1,0]\n\t" \
  "v_pk_mul_f32 v[58:59], v[58:59], v[76:77] op_sel_hi:[1,0]\n\t" \
  "v_pk_mul_f32 v[60:61], v[60:61], v[76:77] op_sel_hi:[1,0]\n\t" \
  "v_pk_mul_f32 v[62:63], v[62:63], v[76:77] op_sel_hi:[1,0]\n\t" \
  "v_pk_mul_f32 v[64:65], v[64:65], v[76:77] op_sel_hi:[1,0]\n\t" \
  "v_pk_mul_f32 v[66:67], v[66:67], v[76:77] op_sel_hi:[1,0]\n\t" \
  "v_pk_mul_f32 v[68:69], v[68:69], v[76:77] op_sel_hi:[1,0]\n\t" \
  "v_pk_mul_f32 v[70:71], v[70:71], v[76:77] op_sel_hi:[1,0]\n\t" \
  "v_pk_fma_f32 v[56:57], v[136:137], v[76:77], v[56:57] op_sel:[0,1,0] op_sel_hi:[1,1,1]\n\t" \
  "v_pk_fma_f32 v[58:59], v[138:139], v[76:77], v[58:59] op_sel:[0,1,0] op_sel_hi:[1,1,1]\n\t" \
  "v_pk_fma_f32 v[60:61], v[140:141], v[76:77], v[60:61] op_sel:[0,1,0] op_sel_hi:[1,1,1]\n\t" \
  "v_pk_fma_f32 v[62:63], v[142:143], v[76:77], v[62:63] op_sel:[0,1,0] op_sel_hi:[1,1,1]\n\t" \
  "v_pk_fma_f32 v[64:65], v[144:145], v[76:77], v[64:65] op_sel:[0,1,0] op_sel_hi:[1,1,1]\n\t" \
  "v_pk_fma_f32 v[66:67], v[146:147], v[76:77], v[66:67] op_sel:[0,1,0] op_sel_hi:[1,1,1]\n\t" \
  "v_pk_fma_f32 v[68:69], v[148:149], v[76:77], v[68:69] op_sel:[0,1,0] op_sel_hi:[1,1,1]\n\t" \
  "v_pk_fma_f32 v[70:71], v[150:151], v[76:77], v[70:71] op_sel:[0,1,0] op_sel_hi:[1,1,1]\n\t" \
  "ds_read_b128 v[136:139], v236 offset:6144\n\t" \
  "ds_read_b128 v[140:143], v236 offset:7168\n\t" \
  "ds_read_b128 v[144:147], v238 offset:0\n\t" \
  "ds_read_b128 v[148:151], v238 offset:1024\n\t" \
  "v_cvt_pk_bf16_f32 v56, v56, v57\n\t" \
  "v_cvt_pk_bf16_f32 v57, v58, v59\n\t" \
  "v_cvt_pk_bf16_f32 v58, v60, v61\n\t" \
  "v_cvt_pk_bf16_f32 v59, v62, v63\n\t" \
  "ds_write_b128 v236, v[56:59] offset:0\n\t" \
  "v_cvt_pk_bf16_f32 v60, v64, v65\n\t" \
  "v_cvt_pk_bf16_f32 v61, v66, v67\n\t" \
  "v_cvt_pk_bf16_f32 v62, v68, v69\n\t" \
  "v_cvt_pk_bf16_f32 v63, v70, v71\n\t" \
  "ds_write_b128 v236, v[60:63] offset:1024\n\t" \
  "s_waitcnt lgkmcnt(10)\n\t" \
  "v_pk_mul_f32 v[188:189], v[188:189], v[80:81] op_sel_hi:[1,0]\n\t" \
  "v_pk_mul_f32 v[190:191], v[190:191], v[80:81] op_sel_hi:[1,0]\n\t" \
  "v_pk_mul_f32 v[192:193], v[192:193], v[80:81] op_sel_hi:[1,0]\n\t" \
  "v_pk_mul_f32 v[194:195], v[194:195], v[80:81] op_sel_hi:[1,0]\n\t" \
  "v_pk_mul_f32 v[196:197], v[196:197], v[80:81] op_sel_hi:[1,0]\n\t" \
  "v_pk_mul_f32 v[198:199], v[198:199], v[80:81] op_sel_hi:[1,0]\n\t" \
  "v_pk_mul_f32 v[200:201], v[200:201], v[80:81] op_sel_hi:[1,0]\n\t" \
  "v_pk_mul_f32 v[202:203], v[202:203], v[80:81] op_sel_hi:[1,0]\n\t" \
  "v_pk_fma_f32 v[188:189], v[152:153], v[80:81], v[188:189] op_sel:[0,1,0] op_sel_hi:[1,1,1]\n\t" \
  "v_pk_fma_f32 v[190:191], v[154:155], v[80:81], v[190:191] op_sel:[0,1,0] op_sel_hi:[1,1,1]\n\t" \
  "v_pk_fma_f32 v[192:193], v[156:157], v[80:81], v[192:193] op_sel:[0,1,0] op_sel_hi:[1,1,1]\n\t" \
  "v_pk_fma_f32 v[194:195], v[158:159], v[80:81], v[194:195] op_sel:[0,1,0] op_sel_hi:[1,1,1]\n\t" \
  "v_pk_fma_f32 v[196:197], v[160:161], v[80:81], v[196:197] op_sel:[0,1,0] op_sel_hi:[1,1,1]\n\t" \
  "v_pk_fma_f32 v[198:199], v[162:163], v[80:81], v[198:199] op_sel:[0,1,0] op_sel_hi:[1,1,1]\n\t" \
  "v_pk_fma_f32 v[200:201], v[164:165], v[80:81], v[200:201] op_sel:[0,1,0] op_sel_hi:[1,1,1]\n\t" \
  "v_pk_fma_f32 v[202:203], v[166:167], v[80:81], v[202:203] op_sel:[0,1,0] op_sel_hi:[1,1,1]\n\t" \
  "ds_read_b128 v[152:155], v237 offset:0\n\t" \
  "ds_read_b128 v[156:159], v237 offset:1024\n\t" \
  "ds_read_b128 v[160:163], v238 offset:2048\n\t" \
  "ds_read_b128 v[164:167], v238 offset:3072\n\t" \
  "v_cvt_pk_bf16_f32 v188, v188, v189\n\t" \
  "v_cvt_pk_bf16_f32 v189, v190, v191\n\t" \
  "v_cvt_pk_bf16_f32 v190, v192, v193\n\t" \
  "v_cvt_pk_bf16_f32 v191, v194, v195\n\t" \
  "ds_write_b128 v236, v[188:191] offset:2048\n\t" \
  "v_cvt_pk_bf16_f32 v192, v196, v197\n\t" \
  "v_cvt_pk_bf16_f32 v193, v198, v199\n\t" \
  "v_cvt_pk_bf16_f32 v194, v200, v201\n\t" \
  "v_cvt_pk_bf16_f32 v195, v202, v203\n\t" \
  "ds_write_b128 v236, v[192:195] offset:3072\n\t" \
  "s_waitcnt lgkmcnt(12)\n\t" \
  "v_pk_mul_f32 v[204:205], v[204:205], v[84:85] op_sel_hi:[1,0]\n\t" \
  "v_pk_mul_f32 v[206:207], v[206:207], v[84:85] op_sel_hi:[1,0]\n\t" \
  "v_pk_mul_f32 v[208:209], v[208:209], v[84:85] op_sel_hi:[1,0]\n\t" \
  "v_pk_mul_f32 v[210:211], v[210:211], v[84:85] op_sel_hi:[1,0]\n\t" \
  "v_pk_mul_f32 v[212:213], v[212:213], v[84:85] op_sel_hi:[1,0]\n\t" \
  "v_pk_mul_f32 v[214:215], v[214:215], v[84:85] op_sel_hi:[1,0]\n\t" \
  "v_pk_mul_f32 v[216:217], v[216:217], v[84:85] op_sel_hi:[1,0]\n\t" \
  "v_pk_mul_f32 v[218:219], v[218:219], v[84:85] op_sel_hi:[1,0]\n\t" \
  "v_pk_fma_f32 v[204:205], v[168:169], v[84:85], v[204:205] op_sel:[0,1,0] op_sel_hi:[1,1,1]\n\t" \
  "v_pk_fma_f32 v[206:207], v[170:171], v[84:85], v[206:207] op_sel:[0,1,0] op_sel_hi:[1,1,1]\n\t" \
  "v_pk_fma_f32 v[208:209], v[172:173], v[84:85], v[208:209] op_sel:[0,1,0] op_sel_hi:[1,1,1]\n\t" \
  "v_pk_fma_f32 v[210:211], v[174:175], v[84:85], v[210:211] op_sel:[0,1,0] op_sel_hi:[1,1,1]\n\t" \
  "v_pk_fma_f32 v[212:213], v[176:177], v[84:85], v[212:213] op_sel:[0,1,0] op_sel_hi:[1,1,1]\n\t" \
  "v_pk_fma_f32 v[214:215], v[178:179], v[84:85], v[214:215] op_sel:[0,1,0] op_sel_hi:[1,1,1]\n\t" \
  "v_pk_fma_f32 v[216:217], v[180:181], v[84:85], v[216:217] op_sel:[0,1,0] op_sel_hi:[1,1,1]\n\t" \
  "v_pk_fma_f32 v[218:219], v[182:183], v[84:85], v[218:219] op_sel:[0,1,0] op_sel_hi:[1,1,1]\n\t" \
  "v_cvt_pk_bf16_f32 v204, v204, v205\n\t" \
  "v_cvt_pk_bf16_f32 v205, v206, v207\n\t" \
  "v_cvt_pk_bf16_f32 v206, v208, v209\n\t" \
  "v_cvt_pk_bf16_f32 v207, v210, v211\n\t" \
  "ds_write_b128 v236, v[204:207] offset:4096\n\t" \
  "v_cvt_pk_bf16_f32 v208, v212, v213\n\t" \
  "v_cvt_pk_bf16_f32 v209, v214, v215\n\t" \
  "v_cvt_pk_bf16_f32 v210, v216, v217\n\t" \
  "v_cvt_pk_bf16_f32 v211, v218, v219\n\t" \
  "ds_write_b128 v236, v[208:211] offset:5120\n\t" \
  "s_waitcnt lgkmcnt(10)\n\t" \
  "v_pk_mul_f32 v[96:97], v[96:97], v[88:89] op_sel_hi:[1,0]\n\t" \
  "v_pk_mul_f32 v[98:99], v[98:99], v[88:89] op_sel_hi:[1,0]\n\t" \
  "v_pk_mul_f32 v[100:101], v[100:101], v[88:89] op_sel_hi:[1,0]\n\t" \
  "v_pk_mul_f32 v[102:103], v[102:103], v[88:89] op_sel_hi:[1,0]\n\t" \
  "v_pk_mul_f32 v[104:105], v[104:105], v[88:89] op_sel_hi:[1,0]\n\t" \
  "v_pk_mul_f32 v[106:107], v[106:107], v[88:89] op_sel_hi:[1,0]\n\t" \
  "v_pk_mul_f32 v[108:109], v[108:109], v[88:89] op_sel_hi:[1,0]\n\t" \
  "v_pk_mul_f32 v[110:111], v[110:111], v[88:89] op_sel_hi:[1,0]\n\t" \
  "v_pk_fma_f32 v[96:97], v[136:137], v[88:89], v[96:97] op_sel:[0,1,0] op_sel_hi:[1,1,1]\n\t" \
  "v_pk_fma_f32 v[98:99], v[138:139], v[88:89], v[98:99] op_sel:[0,1,0] op_sel_hi:[1,1,1]\n\t" \
  "v_pk_fma_f32 v[100:101], v[140:141], v[88:89], v[100:101] op_sel:[0,1,0] op_sel_hi:[1,1,1]\n\t" \
  "v_pk_fma_f32 v[102:103], v[142:143], v[88:89], v[102:103] op_sel:[0,1,0] op_sel_hi:[1,1,1]\n\t" \
  "v_pk_fma_f32 v[104:105], v[144:145], v[88:89], v[104:105] op_sel:[0,1,0] op_sel_hi:[1,1,1]\n\t" \
  "v_pk_fma_f32 v[106:107], v[146:147], v[88:89], v[106:107] op_sel:[0,1,0] op_sel_hi:[1,1,1]\n\t" \
  "v_pk_fma_f32 v[108:109], v[148:149], v[88:89], v[108:109] op_sel:[0,1,0] op_sel_hi:[1,1,1]\n\t" \
  "v_pk_fma_f32 v[110:111], v[150:151], v[88:89], v[110:111] op_sel:[0,1,0] op_sel_hi:[1,1,1]\n\t" \
  "v_cvt_pk_bf16_f32 v96, v96, v97\n\t" \
  "v_cvt_pk_bf16_f32 v97, v98, v99\n\t" \
  "v_cvt_pk_bf16_f32 v98, v100, v101\n\t" \
  "v_cvt_pk_bf16_f32 v99, v102, v103\n\t" \
  "ds_write_b128 v236, v[96:99] offset:6144\n\t" \
  "v_cvt_pk_bf16_f32 v100, v104, v105\n\t" \
  "v_cvt_pk_bf16_f32 v101, v106, v107\n\t" \
  "v_cvt_pk_bf16_f32 v102, v108, v109\n\t" \
  "v_cvt_pk_bf16_f32 v103, v110, v111\n\t" \
  "ds_write_b128 v236, v[100:103] offset:7168\n\t" \
  "s_waitcnt lgkmcnt(6)\n\t" \
  "v_pk_mul_f32 v[112:113], v[112:113], v[92:93] op_sel_hi:[1,0]\n\t" \
  "v_pk_mul_f32 v[114:115], v[114:115], v[92:93] op_sel_hi:[1,0]\n\t" \
  "v_pk_mul_f32 v[116:117], v[116:117], v[92:93] op_sel_hi:[1,0]\n\t" \
  "v_pk_mul_f32 v[118:119], v[118:119], v[92:93] op_sel_hi:[1,0]\n\t" \
  "v_pk_mul_f32 v[120:121], v[120:121], v[92:93] op_sel_hi:[1,0]\n\t" \
  "v_pk_mul_f32 v[122:123], v[122:123], v[92:93] op_sel_hi:[1,0]\n\t" \
  "v_pk_mul_f32 v[124:125], v[124:125], v[92:93] op_sel_hi:[1,0]\n\t" \
  "v_pk_mul_f32 v[126:127], v[126:127], v[92:93] op_sel_hi:[1,0]\n\t" \
  "v_pk_fma_f32 v[112:113], v[152:153], v[92:93], v[112:113] op_sel:[0,1,0] op_sel_hi:[1,1,1]\n\t" \
  "v_pk_fma_f32 v[114:115], v[154:155], v[92:93], v[114:115] op_sel:[0,1,0] op_sel_hi:[1,1,1]\n\t" \
  "v_pk_fma_f32 v[116:117], v[156:157], v[92:93], v[116:117] op_sel:[0,1,0] op_sel_hi:[1,1,1]\n\t" \
  "v_pk_fma_f32 v[118:119], v[158:159], v[92:93], v[118:119] op_sel:[0,1,0] op_sel_hi:[1,1,1]\n\t" \
  "v_pk_fma_f32 v[120:121], v[160:161], v[92:93], v[120:121] op_sel:[0,1,0] op_sel_hi:[1,1,1]\n\t" \
  "v_pk_fma_f32 v[122:123], v[162:163], v[92:93], v[122:123] op_sel:[0,1,0] op_sel_hi:[1,1,1]\n\t" \
  "v_pk_fma_f32 v[124:125], v[164:165], v[92:93], v[124:125] op_sel:[0,1,0] op_sel_hi:[1,1,1]\n\t" \
  "v_pk_fma_f32 v[126:127], v[166:167], v[92:93], v[126:127] op_sel:[0,1,0] op_sel_hi:[1,1,1]\n\t" \
  "v_cvt_pk_bf16_f32 v112, v112, v113\n\t" \
  "v_cvt_pk_bf16_f32 v113, v114, v115\n\t" \
  "v_cvt_pk_bf16_f32 v114, v116, v117\n\t" \
  "v_cvt_pk_bf16_f32 v115, v118, v119\n\t" \
  "ds_write_b128 v237, v[112:115] offset:0\n\t" \
  "v_cvt_pk_bf16_f32 v116, v120, v121\n\t" \
  "v_cvt_pk_bf16_f32 v117, v122, v123\n\t" \
  "v_cvt_pk_bf16_f32 v118, v124, v125\n\t" \
  "v_cvt_pk_bf16_f32 v119, v126, v127\n\t" \
  "ds_write_b128 v237, v[116:119] offset:1024\n\t" \
  "v_subrev_u32 v233, %[w8k], v233\n\t" \
  "v_subrev_u32 v234, %[w8k], v234\n\t" \
  "v_add_u32 v88, s75, %[gtr]\n\t" \
  "v_xor_b32 v89, 32, v88\n\t" \
  "v_xor_b32 v90, 64, v88\n\t" \
  "v_xor_b32 v91, 96, v88\n\t" \
  "s_waitcnt vmcnt(8) lgkmcnt(0)\n\t" \
  "s_barrier\n\t" \
  "ds_read_b128 a[0:3], v233 offset:8192\n\t" \
  "ds_read_b128 a[4:7], v233 offset:10240\n\t" \
  "ds_read_b128 a[8:11], v233 offset:12288\n\t" \
  "ds_read_b128 a[12:15], v233 offset:14336\n\t" \
  "ds_read_b128 a[16:19], v234 offset:8192\n\t" \
  "ds_read_b64_tr_b16 v[56:57], v88 offset:0\n\t" \
  "ds_read_b64_tr_b16 v[58:59], v88 offset:2048\n\t" \
  "ds_read_b64_tr_b16 v[60:61], v89 offset:0\n\t" \
  "ds_read_b64_tr_b16 v[62:63], v89 offset:2048\n\t" \
  "ds_read_b64_tr_b16 v[64:65], v90 offset:0\n\t" \
  "ds_read_b64_tr_b16 v[66:67], v90 offset:2048\n\t" \
  "ds_read_b64_tr_b16 v[68:69], v91 offset:0\n\t" \
  "ds_read_b64_tr_b16 v[70:71], v91 offset:2048\n\t" \
  "s_waitcnt lgkmcnt(0)\n\t" \
  "v_mfma_f32_16x16x32_bf16 v[96:99], v[56:59], a[0:3], 0\n\t" \
  "ds_read_b64_tr_b16 v[72:73], v88 offset:4096\n\t" \
  "ds_read_b128 a[20:23], v233 offset:9216\n\t" \
  "v_mfma_f32_16x16x32_bf16 v[100:103], v[56:59], a[4:7], 0\n\t" \
  "ds_read_b64_tr_b16 v[74:75], v88 offset:6144\n\t" \
  "ds_read_b128 a[24:27], v233 offset:11264\n\t" \
  "v_mfma_f32_16x16x32_bf16 v[104:107], v[56:59], a[8:11], 0\n\t" \
  "s_add_u32 m0, s81, 0\n\t" \
  "s_mov_b32 s71, 131072\n\t" \
  "buffer_load_dwordx4 %[wov], %[srdWo], s71 offen lds\n\t" \
  "ds_read_b64_tr_b16 v[76:77], v89 offset:4096\n\t" \
  "v_mfma_f32_16x16x32_bf16 v[108:111], v[56:59], a[12:15], 0\n\t" \
  "ds_read_b128 a[28:31], v233 offset:13312\n\t" \
  "ds_read_b64_tr_b16 v[78:79], v89 offset:6144\n\t" \
  "v_mfma_f32_16x16x32_bf16 v[112:115], v[56:59], a[16:19], 0\n\t" \
  "ds_read_b128 a[32:35], v233 offset:15360\n\t" \
  "buffer_load_dwordx4 v252, %[srdWo], s71 offen offset:1024 lds\n\t" \
  "v_mfma_f32_16x16x32_bf16 v[116:119], v[60:63], a[0:3], 0\n\t" \
  "ds_read_b64_tr_b16 v[80:81], v90 offset:4096\n\t" \
  "ds_read_b128 a[36:39], v234 offset:9216\n\t" \
  "v_mfma_f32_16x16x32_bf16 v[120:123], v[60:63], a[4:7], 0\n\t" \
  "ds_read_b64_tr_b16 v[82:83], v90 offset:6144\n\t" \
  "ds_read_b128 a[40:43], v233 offset:0\n\t" \
  "v_mfma_f32_16x16x32_bf16 v[124:127], v[60:63], a[8:11], 0\n\t" \
  "buffer_load_dwordx4 v253, %[srdWo], s71 offen offset:2048 lds\n\t" \
  "ds_read_b64_tr_b16 v[84:85], v91 offset:4096\n\t" \
  "v_mfma_f32_16x16x32_bf16 v[128:131], v[60:63], a[12:15], 0\n\t" \
  "ds_read_b128 a[44:47], v233 offset:2048\n\t" \
  "ds_read_b64_tr_b16 v[86:87], v91 offset:6144\n\t" \
  "v_add_u32 v92, s76, %[gtr]\n\t" \
  "v_mfma_f32_16x16x32_bf16 v[132:135], v[60:63], a[16:19], 0\n\t" \
  "ds_read_b128 a[48:51], v233 offset:4096\n\t" \
  "buffer_load_dwordx4 v254, %[srdWo], s71 offen offset:3072 lds\n\t" \
  "v_xor_b32 v93, 32, v92\n\t" \
  "v_mfma_f32_16x16x32_bf16 v[136:139], v[64:67], a[0:3], 0\n\t" \
  "ds_read_b128 a[52:55], v233 offset:6144\n\t" \
  "ds_read_b128 a[56:59], v234 offset:0\n\t" \
  "v_xor_b32 v94, 64, v92\n\t" \
  "v_mfma_f32_16x16x32_bf16 v[140:143], v[64:67], a[4:7], 0\n\t" \
  "ds_read_b128 a[60:63], v233 offset:1024\n\t" \
  "s_add_u32 m0, s81, 4096\n\t" \
  "s_mov_b32 s71, 163840\n\t" \
  "buffer_load_dwordx4 %[wov], %[srdWo], s71 offen lds\n\t" \
  "v_xor_b32 v95, 96, v92\n\t" \
  "v_mfma_f32_16x16x32_bf16 v[144:147], v[64:67], a[8:11], 0\n\t" \
  "ds_read_b128 a[64:67], v233 offset:3072\n\t" \
  "ds_read_b128 a[68:71], v233 offset:5120\n\t" \
  "v_mfma_f32_16x16x32_bf16 v[148:151], v[64:67], a[12:15], 0\n\t" \
  "buffer_load_dwordx4 v252, %[srdWo], s71 offen offset:1024 lds\n\t" \
  "ds_read_b128 a[72:75], v233 offset:7168\n\t" \
  "v_mfma_f32_16x16x32_bf16 v[152:155], v[64:67], a[16:19], 0\n\t" \
  "ds_read_b128 a[76:79], v234 offset:1024\n\t" \
  "buffer_load_dwordx4 v253, %[srdWo], s71 offen offset:2048 lds\n\t" \
  "v_mfma_f32_16x16x32_bf16 v[156:159], v[68:71], a[0:3], 0\n\t" \
  "v_mfma_f32_16x16x32_bf16 v[160:163], v[68:71], a[4:7], 0\n\t" \
  "buffer_load_dwordx4 v254, %[srdWo], s71 offen offset:3072 lds\n\t" \
  "v_mfma_f32_16x16x32_bf16 v[164:167], v[68:71], a[8:11], 0\n\t" \
  "v_mfma_f32_16x16x32_bf16 v[168:171], v[68:71], a[12:15], 0\n\t" \
  "v_mfma_f32_16x16x32_bf16 v[172:175], v[68:71], a[16:19], 0\n\t" \
  "s_waitcnt vmcnt(8) lgkmcnt(0)\n\t" \
  "v_mfma_f32_16x16x32_bf16 v[96:99], v[72:75], a[20:23], v[96:99]\n\t" \
  "ds_read_b64_tr_b16 v[56:57], v92 offset:0\n\t" \
  "ds_read_b128 a[80:83], v233 offset:24576\n\t" \
  "v_mfma_f32_16x16x32_bf16 v[100:103], v[72:75], a[24:27], v[100:103]\n\t" \
  "ds_read_b128 a[84:87], v233 offset:26624\n\t" \
  "ds_read_b64_tr_b16 v[58:59], v92 offset:2048\n\t" \
  "v_mfma_f32_16x16x32_bf16 v[104:107], v[72:75], a[28:31], v[104:107]\n\t" \
  "ds_read_b128 a[88:91], v233 offset:28672\n\t" \
  "s_add_u32 m0, s75, 0\n\t" \
  "s_mov_b32 s71, 196608\n\t" \
  "buffer_load_dwordx4 %[wov], %[srdWo], s71 offen lds\n\t" \
  "v_mfma_f32_16x16x32_bf16 v[108:111], v[72:75], a[32:35], v[108:111]\n\t" \
  "ds_read_b64_tr_b16 v[60:61], v93 offset:0\n\t" \
  "ds_read_b128 a[92:95], v233 offset:30720\n\t" \
  "v_mfma_f32_16x16x32_bf16 v[112:115], v[72:75], a[36:39], v[112:115]\n\t" \
  "ds_read_b64_tr_b16 v[62:63], v93 offset:2048\n\t" \
  "ds_read_b128 a[96:99], v234 offset:24576\n\t" \
  "v_mfma_f32_16x16x32_bf16 v[116:119], v[76:79], a[20:23], v[116:119]\n\t" \
  "ds_read_b128 a[100:103], v233 offset:25600\n\t" \
  "buffer_load_dwordx4 v252, %[srdWo], s71 offen offset:1024 lds\n\t" \
  "v_mfma_f32_16x16x32_bf16 v[120:123], v[76:79], a[24:27], v[120:123]\n\t" \
  "ds_read_b64_tr_b16 v[64:65], v94 offset:0\n\t" \
  "ds_read_b128 a[104:107], v233 offset:27648\n\t" \
  "v_mfma_f32_16x16x32_bf16 v[124:127], v[76:79], a[28:31], v[124:127]\n\t" \
  "ds_read_b64_tr_b16 v[66:67], v94 offset:2048\n\t" \
  "ds_read_b128 a[108:111], v233 offset:29696\n\t" \
  "v_mfma_f32_16x16x32_bf16 v[128:131], v[76:79], a[32:35], v[128:131]\n\t" \
  "buffer_load_dwordx4 v253, %[srdWo], s71 offen offset:2048 lds\n\t" \
  "ds_read_b64_tr_b16 v[68:69], v95 offset:0\n\t" \
  "v_mfma_f32_16x16x32_bf16 v[132:135], v[76:79], a[36:39], v[132:135]\n\t" \
  "ds_read_b128 a[112:115], v233 offset:31744\n\t" \
  "ds_read_b128 a[116:119], v234 offset:25600\n\t" \
  "v_mfma_f32_16x16x32_bf16 v[136:139], v[80:83], a[20:23], v[136:139]\n\t" \
  "ds_read_b64_tr_b16 v[70:71], v95 offset:2048\n\t" \
  "ds_read_b128 a[120:123], v233 offset:16384\n\t" \
  "v_mfma_f32_16x16x32_bf16 v[140:143], v[80:83], a[24:27], v[140:143]\n\t" \
  "buffer_load_dwordx4 v254, %[srdWo], s71 offen offset:3072 lds\n\t" \
  "ds_read_b128 a[124:127], v233 offset:18432\n\t" \
  "v_mfma_f32_16x16x32_bf16 v[144:147], v[80:83], a[28:31], v[144:147]\n\t" \
  "ds_read_b128 a[128:131], v233 offset:20480\n\t" \
  "ds_read_b128 a[132:135], v233 offset:22528\n\t" \
  "v_mfma_f32_16x16x32_bf16 v[148:151], v[80:83], a[32:35], v[148:151]\n\t" \
  "ds_read_b128 a[136:139], v234 offset:16384\n\t" \
  "s_add_u32 m0, s75, 4096\n\t" \
  "s_mov_b32 s71, 229376\n\t" \
  "buffer_load_dwordx4 %[wov], %[srdWo], s71 offen lds\n\t" \
  "v_mfma_f32_16x16x32_bf16 v[152:155], v[80:83], a[36:39], v[152:155]\n\t" \
  "ds_read_b128 a[140:143], v233 offset:17408\n\t" \
  "ds_read_b128 a[144:147], v233 offset:19456\n\t" \
  "v_mfma_f32_16x16x32_bf16 v[156:159], v[84:87], a[20:23], v[156:159]\n\t" \
  "ds_read_b128 a[148:151], v233 offset:21504\n\t" \
  "buffer_load_dwordx4 v252, %[srdWo], s71 offen offset:1024 lds\n\t" \
  "v_mfma_f32_16x16x32_bf16 v[160:163], v[84:87], a[24:27], v[160:163]\n\t" \
  "ds_read_b128 a[152:155], v233 offset:23552\n\t" \
  "ds_read_b128 a[156:159], v234 offset:17408\n\t" \
  "v_mfma_f32_16x16x32_bf16 v[164:167], v[84:87], a[28:31], v[164:167]\n\t" \
  "buffer_load_dwordx4 v253, %[srdWo], s71 offen offset:2048 lds\n\t" \
  "buffer_load_dwordx4 v254, %[srdWo], s71 offen offset:3072 lds\n\t" \
  "v_mfma_f32_16x16x32_bf16 v[168:171], v[84:87], a[32:35], v[168:171]\n\t" \
  "v_mfma_f32_16x16x32_bf16 v[172:175], v[84:87], a[36:39], v[172:175]\n\t" \
  "s_waitcnt lgkmcnt(0)\n\t" \
  "s_barrier\n\t" \
  "v_mfma_f32_16x16x32_bf16 v[96:99], v[56:59], a[40:43], v[96:99]\n\t" \
  "ds_read_b64_tr_b16 v[72:73], v92 offset:4096\n\t" \
  "v_mfma_f32_16x16x32_bf16 v[100:103], v[56:59], a[44:47], v[100:103]\n\t" \
  "ds_read_b64_tr_b16 v[74:75], v92 offset:6144\n\t" \
  "v_mfma_f32_16x16x32_bf16 v[104:107], v[56:59], a[48:51], v[104:107]\n\t" \
  "ds_read_b64_tr_b16 v[76:77], v93 offset:4096\n\t" \
  "v_mfma_f32_16x16x32_bf16 v[108:111], v[56:59], a[52:55], v[108:111]\n\t" \
  "ds_read_b64_tr_b16 v[78:79], v93 offset:6144\n\t" \
  "v_mfma_f32_16x16x32_bf16 v[112:115], v[56:59], a[56:59], v[112:115]\n\t" \
  "ds_read_b64_tr_b16 v[80:81], v94 offset:4096\n\t" \
  "v_mfma_f32_16x16x32_bf16 v[116:119], v[60:63], a[40:43], v[116:119]\n\t" \
  "ds_read_b64_tr_b16 v[82:83], v94 offset:6144\n\t" \
  "v_mfma_f32_16x16x32_bf16 v[120:123], v[60:63], a[44:47], v[120:123]\n\t" \
  "ds_read_b64_tr_b16 v[84:85], v95 offset:4096\n\t" \
  "v_mfma_f32_16x16x32_bf16 v[124:127], v[60:63], a[48:51], v[124:127]\n\t" \
  "ds_read_b64_tr_b16 v[86:87], v95 offset:6144\n\t" \
  "v_mfma_f32_16x16x32_bf16 v[128:131], v[60:63], a[52:55], v[128:131]\n\t" \
  "s_add_u32 m0, s77, 0\n\t" \
  "s_mov_b32 s71, 128\n\t" \
  "buffer_load_dwordx4 %[wov], %[srdWo], s71 offen lds\n\t" \
  "v_add_u32 v88, s81, %[gtr]\n\t" \
  "v_mfma_f32_16x16x32_bf16 v[132:135], v[60:63], a[56:59], v[132:135]\n\t" \
  "buffer_load_dwordx4 v252, %[srdWo], s71 offen offset:1024 lds\n\t" \
  "v_xor_b32 v89, 32, v88\n\t" \
  "v_mfma_f32_16x16x32_bf16 v[136:139], v[64:67], a[40:43], v[136:139]\n\t" \
  "buffer_load_dwordx4 v253, %[srdWo], s71 offen offset:2048 lds\n\t" \
  "v_xor_b32 v90, 64, v88\n\t" \
  "v_mfma_f32_16x16x32_bf16 v[140:143], v[64:67], a[44:47], v[140:143]\n\t" \
  "v_xor_b32 v91, 96, v88\n\t" \
  "v_mfma_f32_16x16x32_bf16 v[144:147], v[64:67], a[48:51], v[144:147]\n\t" \
  "buffer_load_dwordx4 v254, %[srdWo], s71 offen offset:3072 lds\n\t" \
  "v_mfma_f32_16x16x32_bf16 v[148:151], v[64:67], a[52:55], v[148:151]\n\t" \
  "s_add_u32 m0, s77, 4096\n\t" \
  "s_mov_b32 s71, 32896\n\t" \
  "buffer_load_dwordx4 %[wov], %[srdWo], s71 offen lds\n\t" \
  "v_mfma_f32_16x16x32_bf16 v[152:155], v[64:67], a[56:59], v[152:155]\n\t" \
  "buffer_load_dwordx4 v252, %[srdWo], s71 offen offset:1024 lds\n\t" \
  "v_mfma_f32_16x16x32_bf16 v[156:159], v[68:71], a[40:43], v[156:159]\n\t" \
  "v_mfma_f32_16x16x32_bf16 v[160:163], v[68:71], a[44:47], v[160:163]\n\t" \
  "buffer_load_dwordx4 v253, %[srdWo], s71 offen offset:2048 lds\n\t" \
  "v_mfma_f32_16x16x32_bf16 v[164:167], v[68:71], a[48:51], v[164:167]\n\t" \
  "buffer_load_dwordx4 v254, %[srdWo], s71 offen offset:3072 lds\n\t" \
  "v_mfma_f32_16x16x32_bf16 v[168:171], v[68:71], a[52:55], v[168:171]\n\t" \
  "v_mfma_f32_16x16x32_bf16 v[172:175], v[68:71], a[56:59], v[172:175]\n\t" \
  "s_waitcnt vmcnt(16) lgkmcnt(0)\n\t" \
  "v_mfma_f32_16x16x32_bf16 v[96:99], v[72:75], a[60:63], v[96:99]\n\t" \
  "ds_read_b64_tr_b16 v[56:57], v88 offset:0\n\t" \
  "v_mfma_f32_16x16x32_bf16 v[100:103], v[72:75], a[64:67], v[100:103]\n\t" \
  "ds_read_b64_tr_b16 v[58:59], v88 offset:2048\n\t" \
  "v_mfma_f32_16x16x32_bf16 v[104:107], v[72:75], a[68:71], v[104:107]\n\t" \
  "ds_read_b64_tr_b16 v[60:61], v89 offset:0\n\t" \
  "v_mfma_f32_16x16x32_bf16 v[108:111], v[72:75], a[72:75], v[108:111]\n\t" \
  "ds_read_b64_tr_b16 v[62:63], v89 offset:2048\n\t" \
  "v_mfma_f32_16x16x32_bf16 v[112:115], v[72:75], a[76:79], v[112:115]\n\t" \
  "ds_read_b64_tr_b16 v[64:65], v90 offset:0\n\t" \
  "v_mfma_f32_16x16x32_bf16 v[116:119], v[76:79], a[60:63], v[116:119]\n\t" \
  "ds_read_b64_tr_b16 v[66:67], v90 offset:2048\n\t" \
  "v_mfma_f32_16x16x32_bf16 v[120:123], v[76:79], a[64:67], v[120:123]\n\t" \
  "ds_read_b64_tr_b16 v[68:69], v91 offset:0\n\t" \
  "v_mfma_f32_16x16x32_bf16 v[124:127], v[76:79], a[68:71], v[124:127]\n\t" \
  "ds_read_b64_tr_b16 v[70:71], v91 offset:2048\n\t" \
  "v_mfma_f32_16x16x32_bf16 v[128:131], v[76:79], a[72:75], v[128:131]\n\t" \
  "s_add_u32 m0, s80, 0\n\t" \
  "s_mov_b32 s71, 65664\n\t" \
  "buffer_load_dwordx4 %[wov], %[srdWo], s71 offen lds\n\t" \
  "v_mfma_f32_16x16x32_bf16 v[132:135], v[76:79], a[76:79], v[132:135]\n\t" \
  "buffer_load_dwordx4 v252, %[srdWo], s71 offen offset:1024 lds\n\t" \
  "v_mfma_f32_16x16x32_bf16 v[136:139], v[80:83], a[60:63], v[136:139]\n\t" \
  "buffer_load_dwordx4 v253, %[srdWo], s71 offen offset:2048 lds\n\t" \
  "v_mfma_f32_16x16x32_bf16 v[140:143], v[80:83], a[64:67], v[140:143]\n\t" \
  "v_mfma_f32_16x16x32_bf16 v[144:147], v[80:83], a[68:71], v[144:147]\n\t" \
  "buffer_load_dwordx4 v254, %[srdWo], s71 offen offset:3072 lds\n\t" \
  "v_mfma_f32_16x16x32_bf16 v[148:151], v[80:83], a[72:75], v[148:151]\n\t" \
  "s_add_u32 m0, s80, 4096\n\t" \
  "s_mov_b32 s71, 98432\n\t" \
  "buffer_load_dwordx4 %[wov], %[srdWo], s71 offen lds\n\t" \
  "v_mfma_f32_16x16x32_bf16 v[152:155], v[80:83], a[76:79], v[152:155]\n\t" \
  "buffer_load_dwordx4 v252, %[srdWo], s71 offen offset:1024 lds\n\t" \
  "v_mfma_f32_16x16x32_bf16 v[156:159], v[84:87], a[60:63], v[156:159]\n\t" \
  "v_mfma_f32_16x16x32_bf16 v[160:163], v[84:87], a[64:67], v[160:163]\n\t" \
  "buffer_load_dwordx4 v253, %[srdWo], s71 offen offset:2048 lds\n\t" \
  "v_mfma_f32_16x16x32_bf16 v[164:167], v[84:87], a[68:71], v[164:167]\n\t" \
  "buffer_load_dwordx4 v254, %[srdWo], s71 offen offset:3072 lds\n\t" \
  "v_mfma_f32_16x16x32_bf16 v[168:171], v[84:87], a[72:75], v[168:171]\n\t" \
  "v_mfma_f32_16x16x32_bf16 v[172:175], v[84:87], a[76:79], v[172:175]\n\t" \
  "s_waitcnt lgkmcnt(0)\n\t" \
  "v_mfma_f32_16x16x32_bf16 v[96:99], v[56:59], a[80:83], v[96:99]\n\t" \
  "ds_read_b64_tr_b16 v[72:73], v88 offset:4096\n\t" \
  "v_mfma_f32_16x16x32_bf16 v[100:103], v[56:59], a[84:87], v[100:103]\n\t" \
  "ds_read_b64_tr_b16 v[74:75], v88 offset:6144\n\t" \
  "v_mfma_f32_16x16x32_bf16 v[104:107], v[56:59], a[88:91], v[104:107]\n\t" \
  "ds_read_b64_tr_b16 v[76:77], v89 offset:4096\n\t" \
  "v_mfma_f32_16x16x32_bf16 v[108:111], v[56:59], a[92:95], v[108:111]\n\t" \
  "ds_read_b64_tr_b16 v[78:79], v89 offset:6144\n\t" \
  "v_mfma_f32_16x16x32_bf16 v[112:115], v[56:59], a[96:99], v[112:115]\n\t" \
  "ds_read_b64_tr_b16 v[80:81], v90 offset:4096\n\t" \
  "v_mfma_f32_16x16x32_bf16 v[116:119], v[60:63], a[80:83], v[116:119]\n\t" \
  "ds_read_b64_tr_b16 v[82:83], v90 offset:6144\n\t" \
  "v_mfma_f32_16x16x32_bf16 v[120:123], v[60:63], a[84:87], v[120:123]\n\t" \
  "ds_read_b64_tr_b16 v[84:85], v91 offset:4096\n\t" \
  "v_mfma_f32_16x16x32_bf16 v[124:127], v[60:63], a[88:91], v[124:127]\n\t" \
  "ds_read_b64_tr_b16 v[86:87], v91 offset:6144\n\t" \
  "v_mfma_f32_16x16x32_bf16 v[128:131], v[60:63], a[92:95], v[128:131]\n\t" \
  "s_add_u32 m0, s76, 0\n\t" \
  "s_mov_b32 s71, 131200\n\t" \
  "buffer_load_dwordx4 %[wov], %[srdWo], s71 offen lds\n\t" \
  "v_add_u32 v92, s75, %[gtr]\n\t" \
  "v_mfma_f32_16x16x32_bf16 v[132:135], v[60:63], a[96:99], v[132:135]\n\t" \
  "buffer_load_dwordx4 v252, %[srdWo], s71 offen offset:1024 lds\n\t" \
  "v_xor_b32 v93, 32, v92\n\t" \
  "v_mfma_f32_16x16x32_bf16 v[136:139], v[64:67], a[80:83], v[136:139]\n\t" \
  "buffer_load_dwordx4 v253, %[srdWo], s71 offen offset:2048 lds\n\t" \
  "v_xor_b32 v94, 64, v92\n\t" \
  "v_mfma_f32_16x16x32_bf16 v[140:143], v[64:67], a[84:87], v[140:143]\n\t" \
  "v_xor_b32 v95, 96, v92\n\t" \
  "v_mfma_f32_16x16x32_bf16 v[144:147], v[64:67], a[88:91], v[144:147]\n\t" \
  "buffer_load_dwordx4 v254, %[srdWo], s71 offen offset:3072 lds\n\t" \
  "v_mfma_f32_16x16x32_bf16 v[148:151], v[64:67], a[92:95], v[148:151]\n\t" \
  "s_add_u32 m0, s76, 4096\n\t" \
  "s_mov_b32 s71, 163968\n\t" \
  "buffer_load_dwordx4 %[wov], %[srdWo], s71 offen lds\n\t" \
  "v_mfma_f32_16x16x32_bf16 v[152:155], v[64:67], a[96:99], v[152:155]\n\t" \
  "buffer_load_dwordx4 v252, %[srdWo], s71 offen offset:1024 lds\n\t" \
  "v_mfma_f32_16x16x32_bf16 v[156:159], v[68:71], a[80:83], v[156:159]\n\t" \
  "v_mfma_f32_16x16x32_bf16 v[160:163], v[68:71], a[84:87], v[160:163]\n\t" \
  "buffer_load_dwordx4 v253, %[srdWo], s71 offen offset:2048 lds\n\t" \
  "v_mfma_f32_16x16x32_bf16 v[164:167], v[68:71], a[88:91], v[164:167]\n\t" \
  "buffer_load_dwordx4 v254, %[srdWo], s71 offen offset:3072 lds\n\t" \
  "v_mfma_f32_16x16x32_bf16 v[168:171], v[68:71], a[92:95], v[168:171]\n\t" \
  "v_mfma_f32_16x16x32_bf16 v[172:175], v[68:71], a[96:99], v[172:175]\n\t" \
  "s_waitcnt vmcnt(24) lgkmcnt(0)\n\t" \
  "v_mfma_f32_16x16x32_bf16 v[96:99], v[72:75], a[100:103], v[96:99]\n\t" \
  "ds_read_b64_tr_b16 v[56:57], v92 offset:0\n\t" \
  "v_mfma_f32_16x16x32_bf16 v[100:103], v[72:75], a[104:107], v[100:103]\n\t" \
  "ds_read_b64_tr_b16 v[58:59], v92 offset:2048\n\t" \
  "v_mfma_f32_16x16x32_bf16 v[104:107], v[72:75], a[108:111], v[104:107]\n\t" \
  "ds_read_b64_tr_b16 v[60:61], v93 offset:0\n\t" \
  "v_mfma_f32_16x16x32_bf16 v[108:111], v[72:75], a[112:115], v[108:111]\n\t" \
  "ds_read_b64_tr_b16 v[62:63], v93 offset:2048\n\t" \
  "v_mfma_f32_16x16x32_bf16 v[112:115], v[72:75], a[116:119], v[112:115]\n\t" \
  "ds_read_b64_tr_b16 v[64:65], v94 offset:0\n\t" \
  "v_mfma_f32_16x16x32_bf16 v[116:119], v[76:79], a[100:103], v[116:119]\n\t" \
  "ds_read_b64_tr_b16 v[66:67], v94 offset:2048\n\t" \
  "v_mfma_f32_16x16x32_bf16 v[120:123], v[76:79], a[104:107], v[120:123]\n\t" \
  "ds_read_b64_tr_b16 v[68:69], v95 offset:0\n\t" \
  "v_mfma_f32_16x16x32_bf16 v[124:127], v[76:79], a[108:111], v[124:127]\n\t" \
  "ds_read_b64_tr_b16 v[70:71], v95 offset:2048\n\t" \
  "v_mfma_f32_16x16x32_bf16 v[128:131], v[76:79], a[112:115], v[128:131]\n\t" \
  "v_mfma_f32_16x16x32_bf16 v[132:135], v[76:79], a[116:119], v[132:135]\n\t" \
  "v_mfma_f32_16x16x32_bf16 v[136:139], v[80:83], a[100:103], v[136:139]\n\t" \
  "v_mfma_f32_16x16x32_bf16 v[140:143], v[80:83], a[104:107], v[140:143]\n\t" \
  "v_mfma_f32_16x16x32_bf16 v[144:147], v[80:83], a[108:111], v[144:147]\n\t" \
  "v_mfma_f32_16x16x32_bf16 v[148:151], v[80:83], a[112:115], v[148:151]\n\t" \
  "v_mfma_f32_16x16x32_bf16 v[152:155], v[80:83], a[116:119], v[152:155]\n\t" \
  "v_mfma_f32_16x16x32_bf16 v[156:159], v[84:87], a[100:103], v[156:159]\n\t" \
  "v_mfma_f32_16x16x32_bf16 v[160:163], v[84:87], a[104:107], v[160:163]\n\t" \
  "v_mfma_f32_16x16x32_bf16 v[164:167], v[84:87], a[108:111], v[164:167]\n\t" \
  "v_mfma_f32_16x16x32_bf16 v[168:171], v[84:87], a[112:115], v[168:171]\n\t" \
  "v_mfma_f32_16x16x32_bf16 v[172:175], v[84:87], a[116:119], v[172:175]\n\t" \
  "s_waitcnt lgkmcnt(0)\n\t" \
  "v_mfma_f32_16x16x32_bf16 v[96:99], v[56:59], a[120:123], v[96:99]\n\t" \
  "ds_read_b64_tr_b16 v[72:73], v92 offset:4096\n\t" \
  "v_mfma_f32_16x16x32_bf16 v[100:103], v[56:59], a[124:127], v[100:103]\n\t" \
  "ds_read_b64_tr_b16 v[74:75], v92 offset:6144\n\t" \
  "v_mfma_f32_16x16x32_bf16 v[104:107], v[56:59], a[128:131], v[104:107]\n\t" \
  "ds_read_b64_tr_b16 v[76:77], v93 offset:4096\n\t" \
  "v_mfma_f32_16x16x32_bf16 v[108:111], v[56:59], a[132:135], v[108:111]\n\t" \
  "ds_read_b64_tr_b16 v[78:79], v93 offset:6144\n\t" \
  "v_mfma_f32_16x16x32_bf16 v[112:115], v[56:59], a[136:139], v[112:115]\n\t" \
  "ds_read_b64_tr_b16 v[80:81], v94 offset:4096\n\t" \
  "v_mfma_f32_16x16x32_bf16 v[116:119], v[60:63], a[120:123], v[116:119]\n\t" \
  "ds_read_b64_tr_b16 v[82:83], v94 offset:6144\n\t" \
  "v_mfma_f32_16x16x32_bf16 v[120:123], v[60:63], a[124:127], v[120:123]\n\t" \
  "ds_read_b64_tr_b16 v[84:85], v95 offset:4096\n\t" \
  "v_mfma_f32_16x16x32_bf16 v[124:127], v[60:63], a[128:131], v[124:127]\n\t" \
  "ds_read_b64_tr_b16 v[86:87], v95 offset:6144\n\t" \
  "v_mfma_f32_16x16x32_bf16 v[128:131], v[60:63], a[132:135], v[128:131]\n\t" \
  "s_add_u32 m0, s81, 0\n\t" \
  "s_mov_b32 s71, 196736\n\t" \
  "buffer_load_dwordx4 %[wov], %[srdWo], s71 offen lds\n\t" \
  "v_add_u32 v88, s77, %[gtr]\n\t" \
  "v_mfma_f32_16x16x32_bf16 v[132:135], v[60:63], a[136:139], v[132:135]\n\t" \
  "buffer_load_dwordx4 v252, %[srdWo], s71 offen offset:1024 lds\n\t" \
  "v_xor_b32 v89, 32, v88\n\t" \
  "v_mfma_f32_16x16x32_bf16 v[136:139], v[64:67], a[120:123], v[136:139]\n\t" \
  "buffer_load_dwordx4 v253, %[srdWo], s71 offen offset:2048 lds\n\t" \
  "v_xor_b32 v90, 64, v88\n\t" \
  "v_mfma_f32_16x16x32_bf16 v[140:143], v[64:67], a[124:127], v[140:143]\n\t" \
  "v_xor_b32 v91, 96, v88\n\t" \
  "v_mfma_f32_16x16x32_bf16 v[144:147], v[64:67], a[128:131], v[144:147]\n\t" \
  "buffer_load_dwordx4 v254, %[srdWo], s71 offen offset:3072 lds\n\t" \
  "v_mfma_f32_16x16x32_bf16 v[148:151], v[64:67], a[132:135], v[148:151]\n\t" \
  "s_add_u32 m0, s81, 4096\n\t" \
  "s_mov_b32 s71, 229504\n\t" \
  "buffer_load_dwordx4 %[wov], %[srdWo], s71 offen lds\n\t" \
  "v_mfma_f32_16x16x32_bf16 v[152:155], v[64:67], a[136:139], v[152:155]\n\t" \
  "buffer_load_dwordx4 v252, %[srdWo], s71 offen offset:1024 lds\n\t" \
  "v_mfma_f32_16x16x32_bf16 v[156:159], v[68:71], a[120:123], v[156:159]\n\t" \
  "v_mfma_f32_16x16x32_bf16 v[160:163], v[68:71], a[124:127], v[160:163]\n\t" \
  "buffer_load_dwordx4 v253, %[srdWo], s71 offen offset:2048 lds\n\t" \
  "v_mfma_f32_16x16x32_bf16 v[164:167], v[68:71], a[128:131], v[164:167]\n\t" \
  "buffer_load_dwordx4 v254, %[srdWo], s71 offen offset:3072 lds\n\t" \
  "v_mfma_f32_16x16x32_bf16 v[168:171], v[68:71], a[132:135], v[168:171]\n\t" \
  "v_mfma_f32_16x16x32_bf16 v[172:175], v[68:71], a[136:139], v[172:175]\n\t" \
  "s_waitcnt vmcnt(24) lgkmcnt(0)\n\t" \
  "v_mfma_f32_16x16x32_bf16 v[96:99], v[72:75], a[140:143], v[96:99]\n\t" \
  "ds_read_b64_tr_b16 v[56:57], v88 offset:0\n\t" \
  "v_mfma_f32_16x16x32_bf16 v[100:103], v[72:75], a[144:147], v[100:103]\n\t" \
  "ds_read_b64_tr_b16 v[58:59], v88 offset:2048\n\t" \
  "v_mfma_f32_16x16x32_bf16 v[104:107], v[72:75], a[148:151], v[104:107]\n\t" \
  "ds_read_b64_tr_b16 v[60:61], v89 offset:0\n\t" \
  "v_mfma_f32_16x16x32_bf16 v[108:111], v[72:75], a[152:155], v[108:111]\n\t" \
  "ds_read_b64_tr_b16 v[62:63], v89 offset:2048\n\t" \
  "v_mfma_f32_16x16x32_bf16 v[112:115], v[72:75], a[156:159], v[112:115]\n\t" \
  "ds_read_b64_tr_b16 v[64:65], v90 offset:0\n\t" \
  "v_mfma_f32_16x16x32_bf16 v[116:119], v[76:79], a[140:143], v[116:119]\n\t" \
  "ds_read_b64_tr_b16 v[66:67], v90 offset:2048\n\t" \
  "v_mfma_f32_16x16x32_bf16 v[120:123], v[76:79], a[144:147], v[120:123]\n\t" \
  "ds_read_b64_tr_b16 v[68:69], v91 offset:0\n\t" \
  "v_mfma_f32_16x16x32_bf16 v[124:127], v[76:79], a[148:151], v[124:127]\n\t" \
  "ds_read_b64_tr_b16 v[70:71], v91 offset:2048\n\t" \
  "v_mfma_f32_16x16x32_bf16 v[128:131], v[76:79], a[152:155], v[128:131]\n\t" \
  "v_mfma_f32_16x16x32_bf16 v[132:135], v[76:79], a[156:159], v[132:135]\n\t" \
  "v_mfma_f32_16x16x32_bf16 v[136:139], v[80:83], a[140:143], v[136:139]\n\t" \
  "v_mfma_f32_16x16x32_bf16 v[140:143], v[80:83], a[144:147], v[140:143]\n\t" \
  "v_mfma_f32_16x16x32_bf16 v[144:147], v[80:83], a[148:151], v[144:147]\n\t" \
  "v_mfma_f32_16x16x32_bf16 v[148:151], v[80:83], a[152:155], v[148:151]\n\t" \
  "v_mfma_f32_16x16x32_bf16 v[152:155], v[80:83], a[156:159], v[152:155]\n\t" \
  "v_mfma_f32_16x16x32_bf16 v[156:159], v[84:87], a[140:143], v[156:159]\n\t" \
  "v_mfma_f32_16x16x32_bf16 v[160:163], v[84:87], a[144:147], v[160:163]\n\t" \
  "v_mfma_f32_16x16x32_bf16 v[164:167], v[84:87], a[148:151], v[164:167]\n\t" \
  "v_mfma_f32_16x16x32_bf16 v[168:171], v[84:87], a[152:155], v[168:171]\n\t" \
  "v_mfma_f32_16x16x32_bf16 v[172:175], v[84:87], a[156:159], v[172:175]\n\t" \
  "s_waitcnt lgkmcnt(0)\n\t" \
  "v_mfma_f32_16x16x32_bf16 v[176:179], v[56:59], a[0:3], 0\n\t" \
  "ds_read_b64_tr_b16 v[72:73], v88 offset:4096\n\t" \
  "v_mfma_f32_16x16x32_bf16 v[180:183], v[56:59], a[4:7], 0\n\t" \
  "ds_read_b64_tr_b16 v[74:75], v88 offset:6144\n\t" \
  "v_mfma_f32_16x16x32_bf16 v[184:187], v[56:59], a[8:11], 0\n\t" \
  "ds_read_b64_tr_b16 v[76:77], v89 offset:4096\n\t" \
  "v_mfma_f32_16x16x32_bf16 v[188:191], v[56:59], a[12:15], 0\n\t" \
  "ds_read_b64_tr_b16 v[78:79], v89 offset:6144\n\t" \
  "v_mfma_f32_16x16x32_bf16 v[192:195], v[56:59], a[16:19], 0\n\t" \
  "ds_read_b64_tr_b16 v[80:81], v90 offset:4096\n\t" \
  "v_mfma_f32_16x16x32_bf16 v[196:199], v[60:63], a[0:3], 0\n\t" \
  "ds_read_b64_tr_b16 v[82:83], v90 offset:6144\n\t" \
  "v_mfma_f32_16x16x32_bf16 v[200:203], v[60:63], a[4:7], 0\n\t" \
  "ds_read_b64_tr_b16 v[84:85], v91 offset:4096\n\t" \
  "v_mfma_f32_16x16x32_bf16 v[204:207], v[60:63], a[8:11], 0\n\t" \
  "ds_read_b64_tr_b16 v[86:87], v91 offset:6144\n\t" \
  "v_mfma_f32_16x16x32_bf16 v[208:211], v[60:63], a[12:15], 0\n\t" \
  "v_add_u32 v92, s80, %[gtr]\n\t" \
  "v_mfma_f32_16x16x32_bf16 v[212:215], v[60:63], a[16:19], 0\n\t" \
  "v_xor_b32 v93, 32, v92\n\t" \
  "v_mfma_f32_16x16x32_bf16 v[216:219], v[64:67], a[0:3], 0\n\t" \
  "v_xor_b32 v94, 64, v92\n\t" \
  "v_mfma_f32_16x16x32_bf16 v[220:223], v[64:67], a[4:7], 0\n\t" \
  "v_xor_b32 v95, 96, v92\n\t" \
  "v_mfma_f32_16x16x32_bf16 v[224:227], v[64:67], a[8:11], 0\n\t" \
  "v_mfma_f32_16x16x32_bf16 v[228:231], v[64:67], a[12:15], 0\n\t" \
  "v_mfma_f32_16x16x32_bf16 v[232:235], v[64:67], a[16:19], 0\n\t" \
  "v_mfma_f32_16x16x32_bf16 v[236:239], v[68:71], a[0:3], 0\n\t" \
  "v_mfma_f32_16x16x32_bf16 v[240:243], v[68:71], a[4:7], 0\n\t" \
  "v_mfma_f32_16x16x32_bf16 v[244:247], v[68:71], a[8:11], 0\n\t" \
  "v_mfma_f32_16x16x32_bf16 v[248:251], v[68:71], a[12:15], 0\n\t" \
  "v_mfma_f32_16x16x32_bf16 v[252:255], v[68:71], a[16:19], 0\n\t" \
  "s_waitcnt vmcnt(16) lgkmcnt(0)\n\t" \
  "v_mfma_f32_16x16x32_bf16 v[176:179], v[72:75], a[20:23], v[176:179]\n\t" \
  "ds_read_b64_tr_b16 v[56:57], v92 offset:0\n\t" \
  "v_mfma_f32_16x16x32_bf16 v[180:183], v[72:75], a[24:27], v[180:183]\n\t" \
  "ds_read_b64_tr_b16 v[58:59], v92 offset:2048\n\t" \
  "v_cvt_pk_bf16_f32 v96, v96, v97\n\t" \
  "v_mfma_f32_16x16x32_bf16 v[184:187], v[72:75], a[28:31], v[184:187]\n\t" \
  "ds_read_b64_tr_b16 v[60:61], v93 offset:0\n\t" \
  "v_cvt_pk_bf16_f32 v97, v98, v99\n\t" \
  "v_mfma_f32_16x16x32_bf16 v[188:191], v[72:75], a[32:35], v[188:191]\n\t" \
  "ds_read_b64_tr_b16 v[62:63], v93 offset:2048\n\t" \
  "v_cvt_pk_bf16_f32 v98, v116, v117\n\t" \
  "v_mfma_f32_16x16x32_bf16 v[192:195], v[72:75], a[36:39], v[192:195]\n\t" \
  "ds_read_b64_tr_b16 v[64:65], v94 offset:0\n\t" \
  "v_cvt_pk_bf16_f32 v99, v118, v119\n\t" \
  "v_mfma_f32_16x16x32_bf16 v[196:199], v[76:79], a[20:23], v[196:199]\n\t" \
  "ds_read_b64_tr_b16 v[66:67], v94 offset:2048\n\t" \
  "v_mfma_f32_16x16x32_bf16 v[200:203], v[76:79], a[24:27], v[200:203]\n\t" \
  "s_movk_i32 s78, 0\n\t" \
  "buffer_store_dwordx4 v[96:99], %[l16], %[srdP], s78 offen sc1\n\t" \
  "v_mfma_f32_16x16x32_bf16 v[204:207], v[76:79], a[28:31], v[204:207]\n\t" \
  "ds_read_b64_tr_b16 v[68:69], v95 offset:0\n\t" \
  "v_cvt_pk_bf16_f32 v100, v100, v101\n\t" \
  "v_mfma_f32_16x16x32_bf16 v[208:211], v[76:79], a[32:35], v[208:211]\n\t" \
  "ds_read_b64_tr_b16 v[70:71], v95 offset:2048\n\t" \
  "v_cvt_pk_bf16_f32 v101, v102, v103\n\t" \
  "v_mfma_f32_16x16x32_bf16 v[212:215], v[76:79], a[36:39], v[212:215]\n\t" \
  "v_cvt_pk_bf16_f32 v102, v120, v121\n\t" \
  "v_mfma_f32_16x16x32_bf16 v[216:219], v[80:83], a[20:23], v[216:219]\n\t" \
  "v_cvt_pk_bf16_f32 v103, v122, v123\n\t" \
  "v_mfma_f32_16x16x32_bf16 v[220:223], v[80:83], a[24:27], v[220:223]\n\t" \
  "s_movk_i32 s78, 1024\n\t" \
  "buffer_store_dwordx4 v[100:103], %[l16], %[srdP], s78 offen sc1\n\t" \
  "v_mfma_f32_16x16x32_bf16 v[224:227], v[80:83], a[28:31], v[224:227]\n\t" \
  "v_mfma_f32_16x16x32_bf16 v[228:231], v[80:83], a[32:35], v[228:231]\n\t" \
  "v_cvt_pk_bf16_f32 v104, v104, v105\n\t" \
  "v_mfma_f32_16x16x32_bf16 v[232:235], v[80:83], a[36:39], v[232:235]\n\t" \
  "v_cvt_pk_bf16_f32 v105, v106, v107\n\t" \
  "v_mfma_f32_16x16x32_bf16 v[236:239], v[84:87], a[20:23], v[236:239]\n\t" \
  "v_cvt_pk_bf16_f32 v106, v124, v125\n\t" \
  "v_mfma_f32_16x16x32_bf16 v[240:243], v[84:87], a[24:27], v[240:243]\n\t" \
  "v_cvt_pk_bf16_f32 v107, v126, v127\n\t" \
  "v_mfma_f32_16x16x32_bf16 v[244:247], v[84:87], a[28:31], v[244:247]\n\t" \
  "s_movk_i32 s78, 2048\n\t" \
  "buffer_store_dwordx4 v[104:107], %[l16], %[srdP], s78 offen sc1\n\t" \
  "v_mfma_f32_16x16x32_bf16 v[248:251], v[84:87], a[32:35], v[248:251]\n\t" \
  "v_mfma_f32_16x16x32_bf16 v[252:255], v[84:87], a[36:39], v[252:255]\n\t" \
  "s_waitcnt lgkmcnt(0)\n\t" \
  "v_mfma_f32_16x16x32_bf16 v[176:179], v[56:59], a[40:43], v[176:179]\n\t" \
  "ds_read_b64_tr_b16 v[72:73], v92 offset:4096\n\t" \
  "v_mfma_f32_16x16x32_bf16 v[180:183], v[56:59], a[44:47], v[180:183]\n\t" \
  "ds_read_b64_tr_b16 v[74:75], v92 offset:6144\n\t" \
  "v_cvt_pk_bf16_f32 v108, v108, v109\n\t" \
  "v_mfma_f32_16x16x32_bf16 v[184:187], v[56:59], a[48:51], v[184:187]\n\t" \
  "ds_read_b64_tr_b16 v[76:77], v93 offset:4096\n\t" \
  "v_cvt_pk_bf16_f32 v109, v110, v111\n\t" \
  "v_mfma_f32_16x16x32_bf16 v[188:191], v[56:59], a[52:55], v[188:191]\n\t" \
  "ds_read_b64_tr_b16 v[78:79], v93 offset:6144\n\t" \
  "v_cvt_pk_bf16_f32 v110, v128, v129\n\t" \
  "v_mfma_f32_16x16x32_bf16 v[192:195], v[56:59], a[56:59], v[192:195]\n\t" \
  "ds_read_b64_tr_b16 v[80:81], v94 offset:4096\n\t" \
  "v_cvt_pk_bf16_f32 v111, v130, v131\n\t" \
  "v_mfma_f32_16x16x32_bf16 v[196:199], v[60:63], a[40:43], v[196:199]\n\t" \
  "ds_read_b64_tr_b16 v[82:83], v94 offset:6144\n\t" \
  "v_mfma_f32_16x16x32_bf16 v[200:203], v[60:63], a[44:47], v[200:203]\n\t" \
  "s_movk_i32 s78, 3072\n\t" \
  "buffer_store_dwordx4 v[108:111], %[l16], %[srdP], s78 offen sc1\n\t" \
  "v_mfma_f32_16x16x32_bf16 v[204:207], v[60:63], a[48:51], v[204:207]\n\t" \
  "ds_read_b64_tr_b16 v[84:85], v95 offset:4096\n\t" \
  "v_cvt_pk_bf16_f32 v112, v112, v113\n\t" \
  "v_mfma_f32_16x16x32_bf16 v[208:211], v[60:63], a[52:55], v[208:211]\n\t" \
  "ds_read_b64_tr_b16 v[86:87], v95 offset:6144\n\t" \
  "v_add_u32 v88, s76, %[gtr]\n\t" \
  "v_cvt_pk_bf16_f32 v113, v114, v115\n\t" \
  "v_mfma_f32_16x16x32_bf16 v[212:215], v[60:63], a[56:59], v[212:215]\n\t" \
  "v_xor_b32 v89, 32, v88\n\t" \
  "v_cvt_pk_bf16_f32 v114, v132, v133\n\t" \
  "v_mfma_f32_16x16x32_bf16 v[216:219], v[64:67], a[40:43], v[216:219]\n\t" \
  "v_xor_b32 v90, 64, v88\n\t" \
  "v_cvt_pk_bf16_f32 v115, v134, v135\n\t" \
  "v_mfma_f32_16x16x32_bf16 v[220:223], v[64:67], a[44:47], v[220:223]\n\t" \
  "s_movk_i32 s78, 4096\n\t" \
  "buffer_store_dwordx4 v[112:115], %[l16], %[srdP], s78 offen sc1\n\t" \
  "v_xor_b32 v91, 96, v88\n\t" \
  "v_mfma_f32_16x16x32_bf16 v[224:227], v[64:67], a[48:51], v[224:227]\n\t" \
  "v_mfma_f32_16x16x32_bf16 v[228:231], v[64:67], a[52:55], v[228:231]\n\t" \
  "v_cvt_pk_bf16_f32 v136, v136, v137\n\t" \
  "v_mfma_f32_16x16x32_bf16 v[232:235], v[64:67], a[56:59], v[232:235]\n\t" \
  "v_cvt_pk_bf16_f32 v137, v138, v139\n\t" \
  "v_mfma_f32_16x16x32_bf16 v[236:239], v[68:71], a[40:43], v[236:239]\n\t" \
  "v_cvt_pk_bf16_f32 v138, v156, v157\n\t" \
  "v_mfma_f32_16x16x32_bf16 v[240:243], v[68:71], a[44:47], v[240:243]\n\t" \
  "v_cvt_pk_bf16_f32 v139, v158, v159\n\t" \
  "v_mfma_f32_16x16x32_bf16 v[244:247], v[68:71], a[48:51], v[244:247]\n\t" \
  "s_movk_i32 s78, 5120\n\t" \
  "buffer_store_dwordx4 v[136:139], %[l16], %[srdP], s78 offen sc1\n\t" \
  "v_mfma_f32_16x16x32_bf16 v[248:251], v[68:71], a[52:55], v[248:251]\n\t" \
  "v_mfma_f32_16x16x32_bf16 v[252:255], v[68:71], a[56:59], v[252:255]\n\t" \
  "s_waitcnt vmcnt(14) lgkmcnt(0)\n\t" \
  "v_mfma_f32_16x16x32_bf16 v[176:179], v[72:75], a[60:63], v[176:179]\n\t" \
  "ds_read_b64_tr_b16 v[56:57], v88 offset:0\n\t" \
  "v_mfma_f32_16x16x32_bf16 v[180:183], v[72:75], a[64:67], v[180:183]\n\t" \
  "ds_read_b64_tr_b16 v[58:59], v88 offset:2048\n\t" \
  "v_cvt_pk_bf16_f32 v140, v140, v141\n\t" \
  "v_mfma_f32_16x16x32_bf16 v[184:187], v[72:75], a[68:71], v[184:187]\n\t" \
  "ds_read_b64_tr_b16 v[60:61], v89 offset:0\n\t" \
  "v_cvt_pk_bf16_f32 v141, v142, v143\n\t" \
  "v_mfma_f32_16x16x32_bf16 v[188:191], v[72:75], a[72:75], v[188:191]\n\t" \
  "ds_read_b64_tr_b16 v[62:63], v89 offset:2048\n\t" \
  "v_mfma_f32_16x16x32_bf16 v[192:195], v[72:75], a[76:79], v[192:195]\n\t" \
  "ds_read_b64_tr_b16 v[64:65], v90 offset:0\n\t" \
  "v_cvt_pk_bf16_f32 v142, v160, v161\n\t" \
  "v_mfma_f32_16x16x32_bf16 v[196:199], v[76:79], a[60:63], v[196:199]\n\t" \
  "ds_read_b64_tr_b16 v[66:67], v90 offset:2048\n\t" \
  "v_mfma_f32_16x16x32_bf16 v[200:203], v[76:79], a[64:67], v[200:203]\n\t" \
  "ds_read_b64_tr_b16 v[68:69], v91 offset:0\n\t" \
  "v_cvt_pk_bf16_f32 v143, v162, v163\n\t" \
  "v_mfma_f32_16x16x32_bf16 v[204:207], v[76:79], a[68:71], v[204:207]\n\t" \
  "ds_read_b64_tr_b16 v[70:71], v91 offset:2048\n\t" \
  "v_mfma_f32_16x16x32_bf16 v[208:211], v[76:79], a[72:75], v[208:211]\n\t" \
  "s_movk_i32 s78, 6144\n\t" \
  "buffer_store_dwordx4 v[140:143], %[l16], %[srdP], s78 offen sc1\n\t" \
  "v_mfma_f32_16x16x32_bf16 v[212:215], v[76:79], a[76:79], v[212:215]\n\t" \
  "v_mfma_f32_16x16x32_bf16 v[216:219], v[80:83], a[60:63], v[216:219]\n\t" \
  "v_cvt_pk_bf16_f32 v144, v144, v145\n\t" \
  "v_mfma_f32_16x16x32_bf16 v[220:223], v[80:83], a[64:67], v[220:223]\n\t" \
  "v_cvt_pk_bf16_f32 v145, v146, v147\n\t" \
  "v_mfma_f32_16x16x32_bf16 v[224:227], v[80:83], a[68:71], v[224:227]\n\t" \
  "v_mfma_f32_16x16x32_bf16 v[228:231], v[80:83], a[72:75], v[228:231]\n\t" \
  "v_cvt_pk_bf16_f32 v146, v164, v165\n\t" \
  "v_mfma_f32_16x16x32_bf16 v[232:235], v[80:83], a[76:79], v[232:235]\n\t" \
  "v_mfma_f32_16x16x32_bf16 v[236:239], v[84:87], a[60:63], v[236:239]\n\t" \
  "v_cvt_pk_bf16_f32 v147, v166, v167\n\t" \
  "v_mfma_f32_16x16x32_bf16 v[240:243], v[84:87], a[64:67], v[240:243]\n\t" \
  "v_mfma_f32_16x16x32_bf16 v[244:247], v[84:87], a[68:71], v[244:247]\n\t" \
  "s_movk_i32 s78, 7168\n\t" \
  "buffer_store_dwordx4 v[144:147], %[l16], %[srdP], s78 offen sc1\n\t" \
  "v_mfma_f32_16x16x32_bf16 v[248:251], v[84:87], a[72:75], v[248:251]\n\t" \
  "v_mfma_f32_16x16x32_bf16 v[252:255], v[84:87], a[76:79], v[252:255]\n\t" \
  "s_waitcnt lgkmcnt(0)\n\t" \
  "v_mfma_f32_16x16x32_bf16 v[176:179], v[56:59], a[80:83], v[176:179]\n\t" \
  "ds_read_b64_tr_b16 v[72:73], v88 offset:4096\n\t" \
  "v_mfma_f32_16x16x32_bf16 v[180:183], v[56:59], a[84:87], v[180:183]\n\t" \
  "ds_read_b64_tr_b16 v[74:75], v88 offset:6144\n\t" \
  "v_cvt_pk_bf16_f32 v148, v148, v149\n\t" \
  "v_mfma_f32_16x16x32_bf16 v[184:187], v[56:59], a[88:91], v[184:187]\n\t" \
  "ds_read_b64_tr_b16 v[76:77], v89 offset:4096\n\t" \
  "v_cvt_pk_bf16_f32 v149, v150, v151\n\t" \
  "v_mfma_f32_16x16x32_bf16 v[188:191], v[56:59], a[92:95], v[188:191]\n\t" \
  "ds_read_b64_tr_b16 v[78:79], v89 offset:6144\n\t" \
  "v_mfma_f32_16x16x32_bf16 v[192:195], v[56:59], a[96:99], v[192:195]\n\t" \
  "ds_read_b64_tr_b16 v[80:81], v90 offset:4096\n\t" \
  "v_cvt_pk_bf16_f32 v150, v168, v169\n\t" \
  "v_mfma_f32_16x16x32_bf16 v[196:199], v[60:63], a[80:83], v[196:199]\n\t" \
  "ds_read_b64_tr_b16 v[82:83], v90 offset:6144\n\t" \
  "v_mfma_f32_16x16x32_bf16 v[200:203], v[60:63], a[84:87], v[200:203]\n\t" \
  "ds_read_b64_tr_b16 v[84:85], v91 offset:4096\n\t" \
  "v_cvt_pk_bf16_f32 v151, v170, v171\n\t" \
  "v_mfma_f32_16x16x32_bf16 v[204:207], v[60:63], a[88:91], v[204:207]\n\t" \
  "ds_read_b64_tr_b16 v[86:87], v91 offset:6144\n\t" \
  "v_mfma_f32_16x16x32_bf16 v[208:211], v[60:63], a[92:95], v[208:211]\n\t" \
  "s_movk_i32 s78, 8192\n\t" \
  "buffer_store_dwordx4 v[148:151], %[l16], %[srdP], s78 offen sc1\n\t" \
  "v_add_u32 v92, s81, %[gtr]\n\t" \
  "v_mfma_f32_16x16x32_bf16 v[212:215], v[60:63], a[96:99], v[212:215]\n\t" \
  "v_xor_b32 v93, 32, v92\n\t" \
  "v_mfma_f32_16x16x32_bf16 v[216:219], v[64:67], a[80:83], v[216:219]\n\t" \
  "v_xor_b32 v94, 64, v92\n\t" \
  "v_cvt_pk_bf16_f32 v152, v152, v153\n\t" \
  "v_mfma_f32_16x16x32_bf16 v[220:223], v[64:67], a[84:87], v[220:223]\n\t" \
  "v_xor_b32 v95, 96, v92\n\t" \
  "v_cvt_pk_bf16_f32 v153, v154, v155\n\t" \
  "v_mfma_f32_16x16x32_bf16 v[224:227], v[64:67], a[88:91], v[224:227]\n\t" \
  "v_mfma_f32_16x16x32_bf16 v[228:231], v[64:67], a[92:95], v[228:231]\n\t" \
  "v_cvt_pk_bf16_f32 v154, v172, v173\n\t" \
  "v_mfma_f32_16x16x32_bf16 v[232:235], v[64:67], a[96:99], v[232:235]\n\t" \
  "v_mfma_f32_16x16x32_bf16 v[236:239], v[68:71], a[80:83], v[236:239]\n\t" \
  "v_cvt_pk_bf16_f32 v155, v174, v175\n\t" \
  "v_mfma_f32_16x16x32_bf16 v[240:243], v[68:71], a[84:87], v[240:243]\n\t" \
  "v_mfma_f32_16x16x32_bf16 v[244:247], v[68:71], a[88:91], v[244:247]\n\t" \
  "s_movk_i32 s78, 9216\n\t" \
  "buffer_store_dwordx4 v[152:155], %[l16], %[srdP], s78 offen sc1\n\t" \
  "v_mfma_f32_16x16x32_bf16 v[248:251], v[68:71], a[92:95], v[248:251]\n\t" \
  "v_mfma_f32_16x16x32_bf16 v[252:255], v[68:71], a[96:99], v[252:255]\n\t" \
  "s_waitcnt vmcnt(10) lgkmcnt(0)\n\t" \
  "v_mfma_f32_16x16x32_bf16 v[176:179], v[72:75], a[100:103], v[176:179]\n\t" \
  "ds_read_b64_tr_b16 v[56:57], v92 offset:0\n\t" \
  "v_mfma_f32_16x16x32_bf16 v[180:183], v[72:75], a[104:107], v[180:183]\n\t" \
  "ds_read_b64_tr_b16 v[58:59], v92 offset:2048\n\t" \
  "v_mfma_f32_16x16x32_bf16 v[184:187], v[72:75], a[108:111], v[184:187]\n\t" \
  "ds_read_b64_tr_b16 v[60:61], v93 offset:0\n\t" \
  "v_mfma_f32_16x16x32_bf16 v[188:191], v[72:75], a[112:115], v[188:191]\n\t" \
  "ds_read_b64_tr_b16 v[62:63], v93 offset:2048\n\t" \
  "v_mfma_f32_16x16x32_bf16 v[192:195], v[72:75], a[116:119], v[192:195]\n\t" \
  "ds_read_b64_tr_b16 v[64:65], v94 offset:0\n\t" \
  "v_mfma_f32_16x16x32_bf16 v[196:199], v[76:79], a[100:103], v[196:199]\n\t" \
  "ds_read_b64_tr_b16 v[66:67], v94 offset:2048\n\t" \
  "v_mfma_f32_16x16x32_bf16 v[200:203], v[76:79], a[104:107], v[200:203]\n\t" \
  "ds_read_b64_tr_b16 v[68:69], v95 offset:0\n\t" \
  "v_mfma_f32_16x16x32_bf16 v[204:207], v[76:79], a[108:111], v[204:207]\n\t" \
  "ds_read_b64_tr_b16 v[70:71], v95 offset:2048\n\t" \
  "v_mfma_f32_16x16x32_bf16 v[208:211], v[76:79], a[112:115], v[208:211]\n\t" \
  "v_mfma_f32_16x16x32_bf16 v[212:215], v[76:79], a[116:119], v[212:215]\n\t" \
  "v_mfma_f32_16x16x32_bf16 v[216:219], v[80:83], a[100:103], v[216:219]\n\t" \
  "v_mfma_f32_16x16x32_bf16 v[220:223], v[80:83], a[104:107], v[220:223]\n\t" \
  "v_mfma_f32_16x16x32_bf16 v[224:227], v[80:83], a[108:111], v[224:227]\n\t" \
  "v_mfma_f32_16x16x32_bf16 v[228:231], v[80:83], a[112:115], v[228:231]\n\t" \
  "v_mfma_f32_16x16x32_bf16 v[232:235], v[80:83], a[116:119], v[232:235]\n\t" \
  "v_mfma_f32_16x16x32_bf16 v[236:239], v[84:87], a[100:103], v[236:239]\n\t" \
  "v_mfma_f32_16x16x32_bf16 v[240:243], v[84:87], a[104:107], v[240:243]\n\t" \
  "v_mfma_f32_16x16x32_bf16 v[244:247], v[84:87], a[108:111], v[244:247]\n\t" \
  "v_mfma_f32_16x16x32_bf16 v[248:251], v[84:87], a[112:115], v[248:251]\n\t" \
  "v_mfma_f32_16x16x32_bf16 v[252:255], v[84:87], a[116:119], v[252:255]\n\t" \
  "s_waitcnt lgkmcnt(0)\n\t" \
  "v_mfma_f32_16x16x32_bf16 v[176:179], v[56:59], a[120:123], v[176:179]\n\t" \
  "ds_read_b64_tr_b16 v[72:73], v92 offset:4096\n\t" \
  "v_mfma_f32_16x16x32_bf16 v[180:183], v[56:59], a[124:127], v[180:183]\n\t" \
  "ds_read_b64_tr_b16 v[74:75], v92 offset:6144\n\t" \
  "v_mfma_f32_16x16x32_bf16 v[184:187], v[56:59], a[128:131], v[184:187]\n\t" \
  "ds_read_b64_tr_b16 v[76:77], v93 offset:4096\n\t" \
  "v_mfma_f32_16x16x32_bf16 v[188:191], v[56:59], a[132:135], v[188:191]\n\t" \
  "ds_read_b64_tr_b16 v[78:79], v93 offset:6144\n\t" \
  "v_mfma_f32_16x16x32_bf16 v[192:195], v[56:59], a[136:139], v[192:195]\n\t" \
  "ds_read_b64_tr_b16 v[80:81], v94 offset:4096\n\t" \
  "v_mfma_f32_16x16x32_bf16 v[196:199], v[60:63], a[120:123], v[196:199]\n\t" \
  "ds_read_b64_tr_b16 v[82:83], v94 offset:6144\n\t" \
  "v_mfma_f32_16x16x32_bf16 v[200:203], v[60:63], a[124:127], v[200:203]\n\t" \
  "ds_read_b64_tr_b16 v[84:85], v95 offset:4096\n\t" \
  "v_mfma_f32_16x16x32_bf16 v[204:207], v[60:63], a[128:131], v[204:207]\n\t" \
  "ds_read_b64_tr_b16 v[86:87], v95 offset:6144\n\t" \
  "v_mfma_f32_16x16x32_bf16 v[208:211], v[60:63], a[132:135], v[208:211]\n\t" \
  "v_mfma_f32_16x16x32_bf16 v[212:215], v[60:63], a[136:139], v[212:215]\n\t" \
  "v_mfma_f32_16x16x32_bf16 v[216:219], v[64:67], a[120:123], v[216:219]\n\t" \
  "v_mfma_f32_16x16x32_bf16 v[220:223], v[64:67], a[124:127], v[220:223]\n\t" \
  "v_mfma_f32_16x16x32_bf16 v[224:227], v[64:67], a[128:131], v[224:227]\n\t" \
  "v_mfma_f32_16x16x32_bf16 v[228:231], v[64:67], a[132:135], v[228:231]\n\t" \
  "v_mfma_f32_16x16x32_bf16 v[232:235], v[64:67], a[136:139], v[232:235]\n\t" \
  "v_mfma_f32_16x16x32_bf16 v[236:239], v[68:71], a[120:123], v[236:239]\n\t" \
  "v_mfma_f32_16x16x32_bf16 v[240:243], v[68:71], a[124:127], v[240:243]\n\t" \
  "v_mfma_f32_16x16x32_bf16 v[244:247], v[68:71], a[128:131], v[244:247]\n\t" \
  "v_mfma_f32_16x16x32_bf16 v[248:251], v[68:71], a[132:135], v[248:251]\n\t" \
  "v_mfma_f32_16x16x32_bf16 v[252:255], v[68:71], a[136:139], v[252:255]\n\t" \
  "s_waitcnt lgkmcnt(0)\n\t" \
  "v_mfma_f32_16x16x32_bf16 v[176:179], v[72:75], a[140:143], v[176:179]\n\t" \
  "v_mfma_f32_16x16x32_bf16 v[180:183], v[72:75], a[144:147], v[180:183]\n\t" \
  "v_mfma_f32_16x16x32_bf16 v[184:187], v[72:75], a[148:151], v[184:187]\n\t" \
  "v_mfma_f32_16x16x32_bf16 v[188:191], v[72:75], a[152:155], v[188:191]\n\t" \
  "v_mfma_f32_16x16x32_bf16 v[192:195], v[72:75], a[156:159], v[192:195]\n\t" \
  "v_mfma_f32_16x16x32_bf16 v[196:199], v[76:79], a[140:143], v[196:199]\n\t" \
  "v_mfma_f32_16x16x32_bf16 v[200:203], v[76:79], a[144:147], v[200:203]\n\t" \
  "v_mfma_f32_16x16x32_bf16 v[204:207], v[76:79], a[148:151], v[204:207]\n\t" \
  "v_mfma_f32_16x16x32_bf16 v[208:211], v[76:79], a[152:155], v[208:211]\n\t" \
  "v_mfma_f32_16x16x32_bf16 v[212:215], v[76:79], a[156:159], v[212:215]\n\t" \
  "v_mfma_f32_16x16x32_bf16 v[216:219], v[80:83], a[140:143], v[216:219]\n\t" \
  "v_mfma_f32_16x16x32_bf16 v[220:223], v[80:83], a[144:147], v[220:223]\n\t" \
  "v_mfma_f32_16x16x32_bf16 v[224:227], v[80:83], a[148:151], v[224:227]\n\t" \
  "v_mfma_f32_16x16x32_bf16 v[228:231], v[80:83], a[152:155], v[228:231]\n\t" \
  "v_mfma_f32_16x16x32_bf16 v[232:235], v[80:83], a[156:159], v[232:235]\n\t" \
  "v_cvt_pk_bf16_f32 v176, v176, v177\n\t" \
  "v_cvt_pk_bf16_f32 v177, v178, v179\n\t" \
  "v_cvt_pk_bf16_f32 v178, v196, v197\n\t" \
  "v_mfma_f32_16x16x32_bf16 v[236:239], v[84:87], a[140:143], v[236:239]\n\t" \
  "v_cvt_pk_bf16_f32 v179, v198, v199\n\t" \
  "v_mfma_f32_16x16x32_bf16 v[240:243], v[84:87], a[144:147], v[240:243]\n\t" \
  "s_movk_i32 s78, 10240\n\t" \
  "buffer_store_dwordx4 v[176:179], %[l16], %[srdP], s78 offen sc1\n\t" \
  "v_cvt_pk_bf16_f32 v180, v180, v181\n\t" \
  "v_cvt_pk_bf16_f32 v181, v182, v183\n\t" \
  "v_cvt_pk_bf16_f32 v182, v200, v201\n\t" \
  "v_mfma_f32_16x16x32_bf16 v[244:247], v[84:87], a[148:151], v[244:247]\n\t" \
  "v_cvt_pk_bf16_f32 v183, v202, v203\n\t" \
  "v_mfma_f32_16x16x32_bf16 v[248:251], v[84:87], a[152:155], v[248:251]\n\t" \
  "s_movk_i32 s78, 11264\n\t" \
  "buffer_store_dwordx4 v[180:183], %[l16], %[srdP], s78 offen sc1\n\t" \
  "v_cvt_pk_bf16_f32 v184, v184, v185\n\t" \
  "v_cvt_pk_bf16_f32 v185, v186, v187\n\t" \
  "v_cvt_pk_bf16_f32 v186, v204, v205\n\t" \
  "v_mfma_f32_16x16x32_bf16 v[252:255], v[84:87], a[156:159], v[252:255]\n\t" \
  "v_cvt_pk_bf16_f32 v187, v206, v207\n\t" \
  "s_movk_i32 s78, 12288\n\t" \
  "buffer_store_dwordx4 v[184:187], %[l16], %[srdP], s78 offen sc1\n\t" \
  "v_cvt_pk_bf16_f32 v188, v188, v189\n\t" \
  "v_cvt_pk_bf16_f32 v189, v190, v191\n\t" \
  "v_cvt_pk_bf16_f32 v190, v208, v209\n\t" \
  "v_cvt_pk_bf16_f32 v191, v210, v211\n\t" \
  "s_movk_i32 s78, 13312\n\t" \
  "buffer_store_dwordx4 v[188:191], %[l16], %[srdP], s78 offen sc1\n\t" \
  "v_cvt_pk_bf16_f32 v192, v192, v193\n\t" \
  "v_cvt_pk_bf16_f32 v193, v194, v195\n\t" \
  "v_cvt_pk_bf16_f32 v194, v212, v213\n\t" \
  "v_cvt_pk_bf16_f32 v195, v214, v215\n\t" \
  "s_movk_i32 s78, 14336\n\t" \
  "buffer_store_dwordx4 v[192:195], %[l16], %[srdP], s78 offen sc1\n\t" \
  "v_cvt_pk_bf16_f32 v216, v216, v217\n\t" \
  "v_cvt_pk_bf16_f32 v217, v218, v219\n\t" \
  "v_cvt_pk_bf16_f32 v218, v236, v237\n\t" \
  "v_cvt_pk_bf16_f32 v219, v238, v239\n\t" \
  "s_movk_i32 s78, 15360\n\t" \
  "buffer_store_dwordx4 v[216:219], %[l16], %[srdP], s78 offen sc1\n\t" \
  "v_cvt_pk_bf16_f32 v220, v220, v221\n\t" \
  "v_cvt_pk_bf16_f32 v221, v222, v223\n\t" \
  "v_cvt_pk_bf16_f32 v222, v240, v241\n\t" \
  "v_cvt_pk_bf16_f32 v223, v242, v243\n\t" \
  "s_movk_i32 s78, 16384\n\t" \
  "buffer_store_dwordx4 v[220:223], %[l16], %[srdP], s78 offen sc1\n\t" \
  "v_cvt_pk_bf16_f32 v224, v224, v225\n\t" \
  "v_cvt_pk_bf16_f32 v225, v226, v227\n\t" \
  "v_cvt_pk_bf16_f32 v226, v244, v245\n\t" \
  "v_cvt_pk_bf16_f32 v227, v246, v247\n\t" \
  "s_movk_i32 s78, 17408\n\t" \
  "buffer_store_dwordx4 v[224:227], %[l16], %[srdP], s78 offen sc1\n\t" \
  "v_cvt_pk_bf16_f32 v228, v228, v229\n\t" \
  "v_cvt_pk_bf16_f32 v229, v230, v231\n\t" \
  "v_cvt_pk_bf16_f32 v230, v248, v249\n\t" \
  "v_cvt_pk_bf16_f32 v231, v250, v251\n\t" \
  "s_movk_i32 s78, 18432\n\t" \
  "buffer_store_dwordx4 v[228:231], %[l16], %[srdP], s78 offen sc1\n\t" \
  "v_cvt_pk_bf16_f32 v232, v232, v233\n\t" \
  "v_cvt_pk_bf16_f32 v233, v234, v235\n\t" \
  "v_cvt_pk_bf16_f32 v234, v252, v253\n\t" \
  "v_cvt_pk_bf16_f32 v235, v254, v255\n\t" \
  "s_movk_i32 s78, 19456\n\t" \
  "buffer_store_dwordx4 v[232:235], %[l16], %[srdP], s78 offen sc1\n\t" \
  "s_waitcnt vmcnt(0)\n\t" \
  "s_branch .Lend_%=\n\t" \
  ".LloopG_%=:\n\t" \
  "s_waitcnt vmcnt(24) lgkmcnt(0)\n\t" \
  "s_barrier\n\t" \
  "v_mfma_f32_16x16x32_bf16 v[96:99], v[156:159], v[56:59], v[76:79]\n\t" \
  "v_mfma_f32_16x16x32_bf16 v[96:99], v[160:163], a[180:183], v[96:99]\n\t" \
  "s_mov_b32 m0, s69\n\t" \
  "s_xor_b32 s71, s68, s99\n\t" \
  "buffer_load_dwordx4 %[vo0], %[srdV], s71 offen lds\n\t" \
  "v_exp_f32 v108, v108\n\t" \
  "v_mfma_f32_16x16x32_bf16 v[96:99], v[164:167], a[200:203], v[96:99]\n\t" \
  "v_exp_f32 v109, v109\n\t" \
  "v_mfma_f32_16x16x32_bf16 v[96:99], v[168:171], a[220:223], v[96:99]\n\t" \
  "v_add_u32 v220, s66, %[vb]\n\t" \
  "v_mfma_f32_16x16x32_bf16 v[116:119], v[172:175], v[56:59], v[76:79]\n\t" \
  "v_exp_f32 v110, v110\n\t" \
  "v_mfma_f32_16x16x32_bf16 v[116:119], v[176:179], a[180:183], v[116:119]\n\t" \
  "s_add_u32 s68, s68, 8192\n\t" \
  "buffer_load_dwordx4 v255, %[srdV], s71 offen offset:1024 lds\n\t" \
  "v_exp_f32 v111, v111\n\t" \
  "v_mfma_f32_16x16x32_bf16 v[116:119], v[180:183], a[200:203], v[116:119]\n\t" \
  "v_xor_b32 v221, 32, v220\n\t" \
  "v_xor_b32 v222, 64, v220\n\t" \
  "v_mfma_f32_16x16x32_bf16 v[116:119], v[184:187], a[220:223], v[116:119]\n\t" \
  "v_exp_f32 v128, v128\n\t" \
  "v_mfma_f32_16x16x32_bf16 v[100:103], v[156:159], v[60:63], v[80:83]\n\t" \
  "v_exp_f32 v129, v129\n\t" \
  "v_mfma_f32_16x16x32_bf16 v[100:103], v[160:163], a[184:187], v[100:103]\n\t" \
  "buffer_load_dwordx4 %[vo0], %[srdV], s71 offen offset:2048 lds\n\t" \
  "v_xor_b32 v223, 96, v220\n\t" \
  "v_mfma_f32_16x16x32_bf16 v[100:103], v[164:167], a[204:207], v[100:103]\n\t" \
  "v_exp_f32 v130, v130\n\t" \
  "v_mfma_f32_16x16x32_bf16 v[100:103], v[168:171], a[224:227], v[100:103]\n\t" \
  "v_exp_f32 v131, v131\n\t" \
  "v_mfma_f32_16x16x32_bf16 v[120:123], v[172:175], v[60:63], v[80:83]\n\t" \
  "v_xor_b32 v224, 128, v220\n\t" \
  "v_mfma_f32_16x16x32_bf16 v[120:123], v[176:179], a[184:187], v[120:123]\n\t" \
  "buffer_load_dwordx4 v255, %[srdV], s71 offen offset:3072 lds\n\t" \
  "v_exp_f32 v112, v112\n\t" \
  "v_mfma_f32_16x16x32_bf16 v[120:123], v[180:183], a[204:207], v[120:123]\n\t" \
  "v_exp_f32 v113, v113\n\t" \
  "v_mfma_f32_16x16x32_bf16 v[120:123], v[184:187], a[224:227], v[120:123]\n\t" \
  "v_xor_b32 v225, 160, v220\n\t" \
  "v_mfma_f32_16x16x32_bf16 v[104:107], v[156:159], v[64:67], v[84:87]\n\t" \
  "v_xor_b32 v226, 192, v220\n\t" \
  "v_xor_b32 v227, 224, v220\n\t" \
  "v_mfma_f32_16x16x32_bf16 v[104:107], v[160:163], a[188:191], v[104:107]\n\t" \
  "v_exp_f32 v114, v114\n\t" \
  "v_mfma_f32_16x16x32_bf16 v[104:107], v[164:167], a[208:211], v[104:107]\n\t" \
  "ds_read_b64_tr_b16 v[188:189], v220\n\t" \
  "v_cvt_pk_bf16_f32 v148, v108, v109\n\t" \
  "v_mfma_f32_16x16x32_bf16 v[104:107], v[168:171], a[228:231], v[104:107]\n\t" \
  "ds_read_b64_tr_b16 v[190:191], v220 offset:4096\n\t" \
  "v_cvt_pk_bf16_f32 v149, v110, v111\n\t" \
  "v_mfma_f32_16x16x32_bf16 v[124:127], v[172:175], v[64:67], v[84:87]\n\t" \
  "ds_read_b64_tr_b16 v[192:193], v221\n\t" \
  "v_exp_f32 v115, v115\n\t" \
  "v_mfma_f32_16x16x32_bf16 v[124:127], v[176:179], a[188:191], v[124:127]\n\t" \
  "ds_read_b64_tr_b16 v[194:195], v221 offset:4096\n\t" \
  "v_cvt_pk_bf16_f32 v150, v128, v129\n\t" \
  "v_mfma_f32_16x16x32_bf16 v[124:127], v[180:183], a[208:211], v[124:127]\n\t" \
  "ds_read_b64_tr_b16 v[196:197], v222\n\t" \
  "v_cvt_pk_bf16_f32 v151, v130, v131\n\t" \
  "v_mfma_f32_16x16x32_bf16 v[124:127], v[184:187], a[228:231], v[124:127]\n\t" \
  "ds_read_b64_tr_b16 v[198:199], v222 offset:4096\n\t" \
  "v_exp_f32 v132, v132\n\t" \
  "v_mfma_f32_16x16x32_bf16 v[108:111], v[156:159], v[68:71], v[88:91]\n\t" \
  "ds_read_b64_tr_b16 v[200:201], v223\n\t" \
  "v_cvt_pk_bf16_f32 v152, v112, v113\n\t" \
  "v_mfma_f32_16x16x32_bf16 v[108:111], v[160:163], a[192:195], v[108:111]\n\t" \
  "ds_read_b64_tr_b16 v[202:203], v223 offset:4096\n\t" \
  "v_exp_f32 v133, v133\n\t" \
  "v_mfma_f32_16x16x32_bf16 v[108:111], v[164:167], a[212:215], v[108:111]\n\t" \
  "ds_read_b64_tr_b16 v[204:205], v224\n\t" \
  "v_cvt_pk_bf16_f32 v153, v114, v115\n\t" \
  "v_mfma_f32_16x16x32_bf16 v[108:111], v[168:171], a[232:235], v[108:111]\n\t" \
  "ds_read_b64_tr_b16 v[206:207], v224 offset:4096\n\t" \
  "v_exp_f32 v134, v134\n\t" \
  "v_mfma_f32_16x16x32_bf16 v[128:131], v[172:175], v[68:71], v[88:91]\n\t" \
  "ds_read_b64_tr_b16 v[208:209], v225\n\t" \
  "v_exp_f32 v135, v135\n\t" \
  "v_mfma_f32_16x16x32_bf16 v[128:131], v[176:179], a[192:195], v[128:131]\n\t" \
  "ds_read_b64_tr_b16 v[210:211], v225 offset:4096\n\t" \
  "v_cvt_pk_bf16_f32 v154, v132, v133\n\t" \
  "v_mfma_f32_16x16x32_bf16 v[128:131], v[180:183], a[212:215], v[128:131]\n\t" \
  "ds_read_b64_tr_b16 v[212:213], v226\n\t" \
  "v_cvt_pk_bf16_f32 v155, v134, v135\n\t" \
  "v_mfma_f32_16x16x32_bf16 v[128:131], v[184:187], a[232:235], v[128:131]\n\t" \
  "ds_read_b64_tr_b16 v[214:215], v226 offset:4096\n\t" \
  "v_max3_f32 v228, v96, v97, v98\n\t" \
  "v_mfma_f32_16x16x32_bf16 v[112:115], v[156:159], v[72:75], v[92:95]\n\t" \
  "ds_read_b64_tr_b16 v[216:217], v227\n\t" \
  "v_max3_f32 v229, v99, v116, v117\n\t" \
  "v_mfma_f32_16x16x32_bf16 v[112:115], v[160:163], a[196:199], v[112:115]\n\t" \
  "ds_read_b64_tr_b16 v[218:219], v227 offset:4096\n\t" \
  "v_max3_f32 v228, v228, v118, v119\n\t" \
  "v_mfma_f32_16x16x32_bf16 v[112:115], v[164:167], a[216:219], v[112:115]\n\t" \
  "v_max3_f32 v229, v229, v100, v101\n\t" \
  "v_mfma_f32_16x16x32_bf16 v[112:115], v[168:171], a[236:239], v[112:115]\n\t" \
  "v_max3_f32 v228, v228, v102, v103\n\t" \
  "v_max3_f32 v229, v229, v120, v121\n\t" \
  "v_mfma_f32_16x16x32_bf16 v[132:135], v[172:175], v[72:75], v[92:95]\n\t" \
  "v_max3_f32 v228, v228, v122, v123\n\t" \
  "v_max3_f32 v229, v229, v104, v105\n\t" \
  "v_mfma_f32_16x16x32_bf16 v[132:135], v[176:179], a[196:199], v[132:135]\n\t" \
  "v_max3_f32 v228, v228, v106, v107\n\t" \
  "v_mfma_f32_16x16x32_bf16 v[132:135], v[180:183], a[216:219], v[132:135]\n\t" \
  "v_max3_f32 v229, v229, v124, v125\n\t" \
  "v_max3_f32 v228, v228, v126, v127\n\t" \
  "v_mfma_f32_16x16x32_bf16 v[132:135], v[184:187], a[236:239], v[132:135]\n\t" \
  "s_add_u32 s70, s65, s96\n\t" \
  "s_add_u32 s65, s66, s62\n\t" \
  "s_sub_u32 s79, s65, 163840\n\t" \
  "s_cselect_b32 s65, s65, s79\n\t" \
  "v_mfma_f32_16x16x32_bf16 a[160:163], a[240:243], v[136:139], a[160:163]\n\t" \
  "s_waitcnt lgkmcnt(0)\n\t" \
  "v_max3_f32 v229, v229, v108, v109\n\t" \
  "v_mfma_f32_16x16x32_bf16 a[0:3], v[188:191], v[136:139], a[0:3]\n\t" \
  "v_max3_f32 v228, v228, v110, v111\n\t" \
  "v_max3_f32 v229, v229, v128, v129\n\t" \
  "v_mfma_f32_16x16x32_bf16 a[20:23], v[192:195], v[136:139], a[20:23]\n\t" \
  "s_mov_b32 m0, s70\n\t" \
  "s_xor_b32 s71, s67, s99\n\t" \
  "buffer_load_dwordx4 %[ko0], %[srdK], s71 offen lds\n\t" \
  "v_mfma_f32_16x16x32_bf16 a[40:43], v[196:199], v[136:139], a[40:43]\n\t" \
  "v_max3_f32 v228, v228, v130, v131\n\t" \
  "v_max3_f32 v229, v229, v112, v113\n\t" \
  "v_mfma_f32_16x16x32_bf16 a[60:63], v[200:203], v[136:139], a[60:63]\n\t" \
  "v_max3_f32 v228, v228, v114, v115\n\t" \
  "v_max3_f32 v229, v229, v132, v133\n\t" \
  "v_mfma_f32_16x16x32_bf16 a[80:83], v[204:207], v[136:139], a[80:83]\n\t" \
  "v_max3_f32 v228, v228, v134, v135\n\t" \
  "v_max_f32 v228, v228, v229\n\t" \
  "v_mfma_f32_16x16x32_bf16 a[100:103], v[208:211], v[136:139], a[100:103]\n\t" \
  "v_cmp_lt_f32 vcc, s74, v228\n\t" \
  "s_cbranch_vccnz .LtrAG_%=\n\t" \
  ".LbackAG_%=:\n\t" \
  "v_mfma_f32_16x16x32_bf16 a[120:123], v[212:215], v[136:139], a[120:123]\n\t" \
  "v_exp_f32 v96, v96\n\t" \
  "v_mfma_f32_16x16x32_bf16 a[140:143], v[216:219], v[136:139], a[140:143]\n\t" \
  "v_add_u32 v220, s65, %[kb]\n\t" \
  "v_mfma_f32_16x16x32_bf16 a[164:167], a[240:243], v[140:143], a[164:167]\n\t" \
  "v_exp_f32 v97, v97\n\t" \
  "v_mfma_f32_16x16x32_bf16 a[4:7], v[188:191], v[140:143], a[4:7]\n\t" \
  "s_add_u32 s67, s67, 8192\n\t" \
  "buffer_load_dwordx4 v252, %[srdK], s71 offen offset:1024 lds\n\t" \
  "v_exp_f32 v98, v98\n\t" \
  "v_mfma_f32_16x16x32_bf16 a[24:27], v[192:195], v[140:143], a[24:27]\n\t" \
  "v_exp_f32 v99, v99\n\t" \
  "v_mfma_f32_16x16x32_bf16 a[44:47], v[196:199], v[140:143], a[44:47]\n\t" \
  "v_xor_b32 v221, 64, v220\n\t" \
  "v_mfma_f32_16x16x32_bf16 a[64:67], v[200:203], v[140:143], a[64:67]\n\t" \
  "v_exp_f32 v116, v116\n\t" \
  "v_mfma_f32_16x16x32_bf16 a[84:87], v[204:207], v[140:143], a[84:87]\n\t" \
  "v_exp_f32 v117, v117\n\t" \
  "v_mfma_f32_16x16x32_bf16 a[104:107], v[208:211], v[140:143], a[104:107]\n\t" \
  "v_exp_f32 v118, v118\n\t" \
  "v_mfma_f32_16x16x32_bf16 a[124:127], v[212:215], v[140:143], a[124:127]\n\t" \
  "v_xor_b32 v222, 128, v220\n\t" \
  "v_mfma_f32_16x16x32_bf16 a[144:147], v[216:219], v[140:143], a[144:147]\n\t" \
  "v_exp_f32 v119, v119\n\t" \
  "v_mfma_f32_16x16x32_bf16 a[168:171], a[240:243], v[144:147], a[168:171]\n\t" \
  "buffer_load_dwordx4 v253, %[srdK], s71 offen offset:2048 lds\n\t" \
  "v_exp_f32 v100, v100\n\t" \
  "v_mfma_f32_16x16x32_bf16 a[8:11], v[188:191], v[144:147], a[8:11]\n\t" \
  "v_exp_f32 v101, v101\n\t" \
  "v_mfma_f32_16x16x32_bf16 a[28:31], v[192:195], v[144:147], a[28:31]\n\t" \
  "v_xor_b32 v223, 192, v220\n\t" \
  "v_mfma_f32_16x16x32_bf16 a[48:51], v[196:199], v[144:147], a[48:51]\n\t" \
  "v_exp_f32 v102, v102\n\t" \
  "v_mfma_f32_16x16x32_bf16 a[68:71], v[200:203], v[144:147], a[68:71]\n\t" \
  "v_exp_f32 v103, v103\n\t" \
  "v_mfma_f32_16x16x32_bf16 a[88:91], v[204:207], v[144:147], a[88:91]\n\t" \
  "v_exp_f32 v120, v120\n\t" \
  "v_mfma_f32_16x16x32_bf16 a[108:111], v[208:211], v[144:147], a[108:111]\n\t" \
  "v_cvt_pk_bf16_f32 v136, v96, v97\n\t" \
  "v_mfma_f32_16x16x32_bf16 a[128:131], v[212:215], v[144:147], a[128:131]\n\t" \
  "v_exp_f32 v121, v121\n\t" \
  "v_mfma_f32_16x16x32_bf16 a[148:151], v[216:219], v[144:147], a[148:151]\n\t" \
  "v_exp_f32 v122, v122\n\t" \
  "v_mfma_f32_16x16x32_bf16 a[172:175], a[240:243], v[148:151], a[172:175]\n\t" \
  "ds_read_b128 v[156:159], v220\n\t" \
  "v_cvt_pk_bf16_f32 v137, v98, v99\n\t" \
  "v_mfma_f32_16x16x32_bf16 a[12:15], v[188:191], v[148:151], a[12:15]\n\t" \
  "ds_read_b128 v[160:163], v221\n\t" \
  "v_cvt_pk_bf16_f32 v138, v116, v117\n\t" \
  "v_mfma_f32_16x16x32_bf16 a[32:35], v[192:195], v[148:151], a[32:35]\n\t" \
  "v_exp_f32 v123, v123\n\t" \
  "v_exp_f32 v104, v104\n\t" \
  "v_mfma_f32_16x16x32_bf16 a[52:55], v[196:199], v[148:151], a[52:55]\n\t" \
  "ds_read_b128 v[164:167], v222\n\t" \
  "v_cvt_pk_bf16_f32 v139, v118, v119\n\t" \
  "v_mfma_f32_16x16x32_bf16 a[72:75], v[200:203], v[148:151], a[72:75]\n\t" \
  "ds_read_b128 v[168:171], v223\n\t" \
  "v_cvt_pk_bf16_f32 v140, v100, v101\n\t" \
  "v_mfma_f32_16x16x32_bf16 a[92:95], v[204:207], v[148:151], a[92:95]\n\t" \
  "v_exp_f32 v105, v105\n\t" \
  "v_exp_f32 v106, v106\n\t" \
  "v_mfma_f32_16x16x32_bf16 a[112:115], v[208:211], v[148:151], a[112:115]\n\t" \
  "ds_read_b128 v[172:175], v220 offset:4096\n\t" \
  "v_cvt_pk_bf16_f32 v141, v102, v103\n\t" \
  "v_mfma_f32_16x16x32_bf16 a[132:135], v[212:215], v[148:151], a[132:135]\n\t" \
  "ds_read_b128 v[176:179], v221 offset:4096\n\t" \
  "v_cvt_pk_bf16_f32 v142, v120, v121\n\t" \
  "v_mfma_f32_16x16x32_bf16 a[152:155], v[216:219], v[148:151], a[152:155]\n\t" \
  "ds_read_b128 v[180:183], v222 offset:4096\n\t" \
  "v_cvt_pk_bf16_f32 v143, v122, v123\n\t" \
  "v_mfma_f32_16x16x32_bf16 a[176:179], a[240:243], v[152:155], a[176:179]\n\t" \
  "ds_read_b128 v[184:187], v223 offset:4096\n\t" \
  "v_mfma_f32_16x16x32_bf16 a[16:19], v[188:191], v[152:155], a[16:19]\n\t" \
  "v_exp_f32 v107, v107\n\t" \
  "v_exp_f32 v124, v124\n\t" \
  "v_mfma_f32_16x16x32_bf16 a[36:39], v[192:195], v[152:155], a[36:39]\n\t" \
  "buffer_load_dwordx4 v254, %[srdK], s71 offen offset:3072 lds\n\t" \
  "v_cvt_pk_bf16_f32 v144, v104, v105\n\t" \
  "v_mfma_f32_16x16x32_bf16 a[56:59], v[196:199], v[152:155], a[56:59]\n\t" \
  "v_exp_f32 v125, v125\n\t" \
  "v_exp_f32 v126, v126\n\t" \
  "v_mfma_f32_16x16x32_bf16 a[76:79], v[200:203], v[152:155], a[76:79]\n\t" \
  "s_memrealtime s[90:91]\n\t" \
  "v_exp_f32 v127, v127\n\t" \
  "v_mfma_f32_16x16x32_bf16 a[96:99], v[204:207], v[152:155], a[96:99]\n\t" \
  "v_cvt_pk_bf16_f32 v145, v106, v107\n\t" \
  "v_mfma_f32_16x16x32_bf16 a[116:119], v[208:211], v[152:155], a[116:119]\n\t" \
  "v_cvt_pk_bf16_f32 v146, v124, v125\n\t" \
  "v_cvt_pk_bf16_f32 v147, v126, v127\n\t" \
  "v_mfma_f32_16x16x32_bf16 a[136:139], v[212:215], v[152:155], a[136:139]\n\t" \
  "s_add_u32 s69, s66, s96\n\t" \
  "s_add_u32 s66, s65, s62\n\t" \
  "s_sub_u32 s79, s66, 163840\n\t" \
  "s_cselect_b32 s66, s66, s79\n\t" \
  "v_mfma_f32_16x16x32_bf16 a[156:159], v[216:219], v[152:155], a[156:159]\n\t" \
  "s_cmp_lg_u32 s73, 0\n\t" \
  "s_cbranch_scc1 .LtrBG_%=\n\t" \
  ".LbackBG_%=:\n\t" \
  "s_waitcnt lgkmcnt(0)\n\t" \
  "s_sub_u32 s90, s92, s90\n\t" \
  "s_add_u32 s92, s92, 93\n\t" \
  "s_min_i32 s90, s90, 40\n\t" \
  ".Lpace_%=:\n\t" \
  "s_sub_i32 s90, s90, 4\n\t" \
  "s_cmp_lt_i32 s90, 0\n\t" \
  "s_cbranch_scc1 .Lpaced_%=\n\t" \
  "s_sleep 1\n\t" \
  "s_branch .Lpace_%=\n\t" \
  ".Lpaced_%=:\n\t" \
  "s_cmp_ge_u32 s68, 122880\n\t" \
  "s_cbranch_scc1 .Lwot_%=\n\t" \
  ".Lwotb_%=:\n\t" \
  "s_cmp_lt_u32 s68, 147456\n\t" \
  "s_cbranch_scc1 .LloopG_%=\n\t" \
  "s_branch .Llast_%=\n\t" \
  ".Lwot_%=:\n\t" \
  "s_bfe_u32 s56, s68, 0x5000a\n\t" \
  "v_lshl_add_u64 v[248:249], s[56:57], 4, %[wtp]\n\t" \
  "global_load_dword v245, v[248:249], off\n\t" \
  "s_branch .Lwotb_%=\n\t" \
  ".LfMK_%=:\n\t" \
  "s_mov_b32 m0, s95\n\t" \
  "s_xor_b32 s71, s67, s99\n\t" \
  "buffer_load_dwordx4 %[ko0], %[srdK], s71 offen nt lds\n\t" \
  "s_add_u32 s67, s67, 8192\n\t" \
  "buffer_load_dwordx4 v252, %[srdK], s71 offen offset:1024 nt lds\n\t" \
  "buffer_load_dwordx4 v253, %[srdK], s71 offen offset:2048 nt lds\n\t" \
  "buffer_load_dwordx4 v254, %[srdK], s71 offen offset:3072 nt lds\n\t" \
  "s_add_u32 m0, s95, 4096\n\t" \
  "s_or_b32 s71, s71, 4096\n\t" \
  "buffer_load_dwordx4 %[ko0], %[srdK], s71 offen nt lds\n\t" \
  "buffer_load_dwordx4 v252, %[srdK], s71 offen offset:1024 nt lds\n\t" \
  "buffer_load_dwordx4 v253, %[srdK], s71 offen offset:2048 nt lds\n\t" \
  "buffer_load_dwordx4 v254, %[srdK], s71 offen offset:3072 nt lds\n\t" \
  "s_add_u32 s95, s95, s62\n\t" \
  "s_sub_u32 s79, s95, 163840\n\t" \
  "s_cselect_b32 s95, s95, s79\n\t" \
  "s_setpc_b64 s[60:61]\n\t" \
  ".LfMV_%=:\n\t" \
  "s_mov_b32 m0, s95\n\t" \
  "s_xor_b32 s71, s68, s99\n\t" \
  "buffer_load_dwordx4 %[vo0], %[srdV], s71 offen nt lds\n\t" \
  "s_add_u32 s68, s68, 8192\n\t" \
  "buffer_load_dwordx4 v255, %[srdV], s71 offen offset:1024 nt lds\n\t" \
  "buffer_load_dwordx4 %[vo0], %[srdV], s71 offen offset:2048 nt lds\n\t" \
  "buffer_load_dwordx4 v255, %[srdV], s71 offen offset:3072 nt lds\n\t" \
  "s_add_u32 m0, s95, 4096\n\t" \
  "s_or_b32 s71, s71, 4096\n\t" \
  "buffer_load_dwordx4 %[vo0], %[srdV], s71 offen nt lds\n\t" \
  "buffer_load_dwordx4 v255, %[srdV], s71 offen offset:1024 nt lds\n\t" \
  "buffer_load_dwordx4 %[vo0], %[srdV], s71 offen offset:2048 nt lds\n\t" \
  "buffer_load_dwordx4 v255, %[srdV], s71 offen offset:3072 nt lds\n\t" \
  "s_add_u32 s95, s95, s62\n\t" \
  "s_sub_u32 s79, s95, 163840\n\t" \
  "s_cselect_b32 s95, s95, s79\n\t" \
  "s_setpc_b64 s[60:61]\n\t" \
  ".LfGK_%=:\n\t" \
  "s_mov_b32 m0, s95\n\t" \
  "s_xor_b32 s71, s67, s99\n\t" \
  "buffer_load_dwordx4 %[ko0], %[srdK], s71 offen lds\n\t" \
  "s_add_u32 s67, s67, 8192\n\t" \
  "buffer_load_dwordx4 v252, %[srdK], s71 offen offset:1024 lds\n\t" \
  "buffer_load_dwordx4 v253, %[srdK], s71 offen offset:2048 lds\n\t" \
  "buffer_load_dwordx4 v254, %[srdK], s71 offen offset:3072 lds\n\t" \
  "s_add_u32 s95, s95, s62\n\t" \
  "s_sub_u32 s79, s95, 163840\n\t" \
  "s_cselect_b32 s95, s95, s79\n\t" \
  "s_setpc_b64 s[60:61]\n\t" \
  ".LfGV_%=:\n\t" \
  "s_mov_b32 m0, s95\n\t" \
  "s_xor_b32 s71, s68, s99\n\t" \
  "buffer_load_dwordx4 %[vo0], %[srdV], s71 offen lds\n\t" \
  "s_add_u32 s68, s68, 8192\n\t" \
  "buffer_load_dwordx4 v255, %[srdV], s71 offen offset:1024 lds\n\t" \
  "buffer_load_dwordx4 %[vo0], %[srdV], s71 offen offset:2048 lds\n\t" \
  "buffer_load_dwordx4 v255, %[srdV], s71 offen offset:3072 lds\n\t" \
  "s_add_u32 s95, s95, s62\n\t" \
  "s_sub_u32 s79, s95, 163840\n\t" \
  "s_cselect_b32 s95, s95, s79\n\t" \
  "s_setpc_b64 s[60:61]\n\t" \
  ".LtrAM_%=:\n\t" \
  "s_getpc_b64 s[60:61]\n\t" \
  "s_branch .LrareA_%=\n\t" \
  "s_branch .LbackAM_%=\n\t" \
  ".LtrAG_%=:\n\t" \
  "s_getpc_b64 s[60:61]\n\t" \
  "s_branch .LrareA_%=\n\t" \
  "s_branch .LbackAG_%=\n\t" \
  ".LtrAL_%=:\n\t" \
  "s_getpc_b64 s[60:61]\n\t" \
  "s_branch .LrareA_%=\n\t" \
  "s_branch .LbackAL_%=\n\t" \
  ".LtrBM_%=:\n\t" \
  "s_getpc_b64 s[60:61]\n\t" \
  "s_branch .LrareB_%=\n\t" \
  "s_branch .LbackBM_%=\n\t" \
  ".LtrBG_%=:\n\t" \
  "s_getpc_b64 s[60:61]\n\t" \
  "s_branch .LrareB_%=\n\t" \
  "s_branch .LbackBG_%=\n\t" \
  ".LtrBL_%=:\n\t" \
  "s_getpc_b64 s[60:61]\n\t" \
  "s_branch .LrareB_%=\n\t" \
  "s_branch .LbackBL_%=\n\t" \
  ".LrareA_%=:\n\t" \
  "v_max3_f32 v228, v96, v97, v98\n\t" \
  "v_max3_f32 v233, v99, v116, v117\n\t" \
  "v_max3_f32 v228, v228, v118, v119\n\t" \
  "v_max_f32 v228, v228, v233\n\t" \
  "v_max3_f32 v229, v100, v101, v102\n\t" \
  "v_max3_f32 v234, v103, v120, v121\n\t" \
  "v_max3_f32 v229, v229, v122, v123\n\t" \
  "v_max_f32 v229, v229, v234\n\t" \
  "v_max3_f32 v230, v104, v105, v106\n\t" \
  "v_max3_f32 v233, v107, v124, v125\n\t" \
  "v_max3_f32 v230, v230, v126, v127\n\t" \
  "v_max_f32 v230, v230, v233\n\t" \
  "v_max3_f32 v231, v108, v109, v110\n\t" \
  "v_max3_f32 v234, v111, v128, v129\n\t" \
  "v_max3_f32 v231, v231, v130, v131\n\t" \
  "v_max_f32 v231, v231, v234\n\t" \
  "v_max3_f32 v232, v112, v113, v114\n\t" \
  "v_max3_f32 v233, v115, v132, v133\n\t" \
  "v_max3_f32 v232, v232, v134, v135\n\t" \
  "v_max_f32 v232, v232, v233\n\t" \
  "v_mov_b32 v233, v228\n\t" \
  "v_mov_b32 v234, v229\n\t" \
  "v_mov_b32 v235, v230\n\t" \
  "v_mov_b32 v236, v231\n\t" \
  "v_mov_b32 v237, v232\n\t" \
  "s_nop 1\n\t" \
  "v_permlane16_swap_b32 v228, v233\n\t" \
  "v_permlane16_swap_b32 v229, v234\n\t" \
  "v_permlane16_swap_b32 v230, v235\n\t" \
  "v_permlane16_swap_b32 v231, v236\n\t" \
  "v_permlane16_swap_b32 v232, v237\n\t" \
  "s_nop 1\n\t" \
  "v_max_f32 v228, v228, v233\n\t" \
  "v_max_f32 v229, v229, v234\n\t" \
  "v_max_f32 v230, v230, v235\n\t" \
  "v_max_f32 v231, v231, v236\n\t" \
  "v_max_f32 v232, v232, v237\n\t" \
  "v_mov_b32 v233, v228\n\t" \
  "v_mov_b32 v234, v229\n\t" \
  "v_mov_b32 v235, v230\n\t" \
  "v_mov_b32 v236, v231\n\t" \
  "v_mov_b32 v237, v232\n\t" \
  "s_nop 1\n\t" \
  "v_permlane32_swap_b32 v228, v233\n\t" \
  "v_permlane32_swap_b32 v229, v234\n\t" \
  "v_permlane32_swap_b32 v230, v235\n\t" \
  "v_permlane32_swap_b32 v231, v236\n\t" \
  "v_permlane32_swap_b32 v232, v237\n\t" \
  "s_nop 1\n\t" \
  "v_max_f32 v228, v228, v233\n\t" \
  "v_max_f32 v229, v229, v234\n\t" \
  "v_max_f32 v230, v230, v235\n\t" \
  "v_max_f32 v231, v231, v236\n\t" \
  "v_max_f32 v232, v232, v237\n\t" \
  "v_max_f32 v228, 0, v228\n\t" \
  "v_sub_f32 v96, v96, v228\n\t" \
  "v_sub_f32 v97, v97, v228\n\t" \
  "v_sub_f32 v98, v98, v228\n\t" \
  "v_sub_f32 v99, v99, v228\n\t" \
  "v_sub_f32 v116, v116, v228\n\t" \
  "v_sub_f32 v117, v117, v228\n\t" \
  "v_sub_f32 v118, v118, v228\n\t" \
  "v_sub_f32 v119, v119, v228\n\t" \
  "v_sub_f32 v76, v76, v228\n\t" \
  "v_sub_f32 v77, v77, v228\n\t" \
  "v_sub_f32 v78, v78, v228\n\t" \
  "v_sub_f32 v79, v79, v228\n\t" \
  "v_exp_f32 v240, -v228\n\t" \
  "v_max_f32 v229, 0, v229\n\t" \
  "v_sub_f32 v100, v100, v229\n\t" \
  "v_sub_f32 v101, v101, v229\n\t" \
  "v_sub_f32 v102, v102, v229\n\t" \
  "v_sub_f32 v103, v103, v229\n\t" \
  "v_sub_f32 v120, v120, v229\n\t" \
  "v_sub_f32 v121, v121, v229\n\t" \
  "v_sub_f32 v122, v122, v229\n\t" \
  "v_sub_f32 v123, v123, v229\n\t" \
  "v_sub_f32 v80, v80, v229\n\t" \
  "v_sub_f32 v81, v81, v229\n\t" \
  "v_sub_f32 v82, v82, v229\n\t" \
  "v_sub_f32 v83, v83, v229\n\t" \
  "v_exp_f32 v241, -v229\n\t" \
  "v_max_f32 v230, 0, v230\n\t" \
  "v_sub_f32 v104, v104, v230\n\t" \
  "v_sub_f32 v105, v105, v230\n\t" \
  "v_sub_f32 v106, v106, v230\n\t" \
  "v_sub_f32 v107, v107, v230\n\t" \
  "v_sub_f32 v124, v124, v230\n\t" \
  "v_sub_f32 v125, v125, v230\n\t" \
  "v_sub_f32 v126, v126, v230\n\t" \
  "v_sub_f32 v127, v127, v230\n\t" \
  "v_sub_f32 v84, v84, v230\n\t" \
  "v_sub_f32 v85, v85, v230\n\t" \
  "v_sub_f32 v86, v86, v230\n\t" \
  "v_sub_f32 v87, v87, v230\n\t" \
  "v_exp_f32 v242, -v230\n\t" \
  "v_max_f32 v231, 0, v231\n\t" \
  "v_sub_f32 v108, v108, v231\n\t" \
  "v_sub_f32 v109, v109, v231\n\t" \
  "v_sub_f32 v110, v110, v231\n\t" \
  "v_sub_f32 v111, v111, v231\n\t" \
  "v_sub_f32 v128, v128, v231\n\t" \
  "v_sub_f32 v129, v129, v231\n\t" \
  "v_sub_f32 v130, v130, v231\n\t" \
  "v_sub_f32 v131, v131, v231\n\t" \
  "v_sub_f32 v88, v88, v231\n\t" \
  "v_sub_f32 v89, v89, v231\n\t" \
  "v_sub_f32 v90, v90, v231\n\t" \
  "v_sub_f32 v91, v91, v231\n\t" \
  "v_exp_f32 v243, -v231\n\t" \
  "v_max_f32 v232, 0, v232\n\t" \
  "v_sub_f32 v112, v112, v232\n\t" \
  "v_sub_f32 v113, v113, v232\n\t" \
  "v_sub_f32 v114, v114, v232\n\t" \
  "v_sub_f32 v115, v115, v232\n\t" \
  "v_sub_f32 v132, v132, v232\n\t" \
  "v_sub_f32 v133, v133, v232\n\t" \
  "v_sub_f32 v134, v134, v232\n\t" \
  "v_sub_f32 v135, v135, v232\n\t" \
  "v_sub_f32 v92, v92, v232\n\t" \
  "v_sub_f32 v93, v93, v232\n\t" \
  "v_sub_f32 v94, v94, v232\n\t" \
  "v_sub_f32 v95, v95, v232\n\t" \
  "v_exp_f32 v244, -v232\n\t" \
  "s_mov_b32 s73, 1\n\t" \
  "s_add_u32 s60, s60, 4\n\t" \
  "s_addc_u32 s61, s61, 0\n\t" \
  "s_setpc_b64 s[60:61]\n\t" \
  ".LrareB_%=:\n\t" \
  "s_nop 7\n\t" \
  "v_accvgpr_read_b32 v233, a160\n\t" \
  "v_accvgpr_read_b32 v234, a161\n\t" \
  "v_accvgpr_read_b32 v235, a162\n\t" \
  "v_accvgpr_read_b32 v236, a163\n\t" \
  "v_mul_f32 v233, v233, v240\n\t" \
  "v_mul_f32 v234, v234, v240\n\t" \
  "v_mul_f32 v235, v235, v240\n\t" \
  "v_mul_f32 v236, v236, v240\n\t" \
  "v_accvgpr_write_b32 a160, v233\n\t" \
  "v_accvgpr_write_b32 a161, v234\n\t" \
  "v_accvgpr_write_b32 a162, v235\n\t" \
  "v_accvgpr_write_b32 a163, v236\n\t" \
  "v_accvgpr_read_b32 v233, a0\n\t" \
  "v_accvgpr_read_b32 v234, a1\n\t" \
  "v_accvgpr_read_b32 v235, a2\n\t" \
  "v_accvgpr_read_b32 v236, a3\n\t" \
  "v_mul_f32 v233, v233, v240\n\t" \
  "v_mul_f32 v234, v234, v240\n\t" \
  "v_mul_f32 v235, v235, v240\n\t" \
  "v_mul_f32 v236, v236, v240\n\t" \
  "v_accvgpr_write_b32 a0, v233\n\t" \
  "v_accvgpr_write_b32 a1, v234\n\t" \
  "v_accvgpr_write_b32 a2, v235\n\t" \
  "v_accvgpr_write_b32 a3, v236\n\t" \
  "v_accvgpr_read_b32 v233, a20\n\t" \
  "v_accvgpr_read_b32 v234, a21\n\t" \
  "v_accvgpr_read_b32 v235, a22\n\t" \
  "v_accvgpr_read_b32 v236, a23\n\t" \
  "v_mul_f32 v233, v233, v240\n\t" \
  "v_mul_f32 v234, v234, v240\n\t" \
  "v_mul_f32 v235, v235, v240\n\t" \
  "v_mul_f32 v236, v236, v240\n\t" \
  "v_accvgpr_write_b32 a20, v233\n\t" \
  "v_accvgpr_write_b32 a21, v234\n\t" \
  "v_accvgpr_write_b32 a22, v235\n\t" \
  "v_accvgpr_write_b32 a23, v236\n\t" \
  "v_accvgpr_read_b32 v233, a40\n\t" \
  "v_accvgpr_read_b32 v234, a41\n\t" \
  "v_accvgpr_read_b32 v235, a42\n\t" \
  "v_accvgpr_read_b32 v236, a43\n\t" \
  "v_mul_f32 v233, v233, v240\n\t" \
  "v_mul_f32 v234, v234, v240\n\t" \
  "v_mul_f32 v235, v235, v240\n\t" \
  "v_mul_f32 v236, v236, v240\n\t" \
  "v_accvgpr_write_b32 a40, v233\n\t" \
  "v_accvgpr_write_b32 a41, v234\n\t" \
  "v_accvgpr_write_b32 a42, v235\n\t" \
  "v_accvgpr_write_b32 a43, v236\n\t" \
  "v_accvgpr_read_b32 v233, a60\n\t" \
  "v_accvgpr_read_b32 v234, a61\n\t" \
  "v_accvgpr_read_b32 v235, a62\n\t" \
  "v_accvgpr_read_b32 v236, a63\n\t" \
  "v_mul_f32 v233, v233, v240\n\t" \
  "v_mul_f32 v234, v234, v240\n\t" \
  "v_mul_f32 v235, v235, v240\n\t" \
  "v_mul_f32 v236, v236, v240\n\t" \
  "v_accvgpr_write_b32 a60, v233\n\t" \
  "v_accvgpr_write_b32 a61, v234\n\t" \
  "v_accvgpr_write_b32 a62, v235\n\t" \
  "v_accvgpr_write_b32 a63, v236\n\t" \
  "v_accvgpr_read_b32 v233, a80\n\t" \
  "v_accvgpr_read_b32 v234, a81\n\t" \
  "v_accvgpr_read_b32 v235, a82\n\t" \
  "v_accvgpr_read_b32 v236, a83\n\t" \
  "v_mul_f32 v233, v233, v240\n\t" \
  "v_mul_f32 v234, v234, v240\n\t" \
  "v_mul_f32 v235, v235, v240\n\t" \
  "v_mul_f32 v236, v236, v240\n\t" \
  "v_accvgpr_write_b32 a80, v233\n\t" \
  "v_accvgpr_write_b32 a81, v234\n\t" \
  "v_accvgpr_write_b32 a82, v235\n\t" \
  "v_accvgpr_write_b32 a83, v236\n\t" \
  "v_accvgpr_read_b32 v233, a100\n\t" \
  "v_accvgpr_read_b32 v234, a101\n\t" \
  "v_accvgpr_read_b32 v235, a102\n\t" \
  "v_accvgpr_read_b32 v236, a103\n\t" \
  "v_mul_f32 v233, v233, v240\n\t" \
  "v_mul_f32 v234, v234, v240\n\t" \
  "v_mul_f32 v235, v235, v240\n\t" \
  "v_mul_f32 v236, v236, v240\n\t" \
  "v_accvgpr_write_b32 a100, v233\n\t" \
  "v_accvgpr_write_b32 a101, v234\n\t" \
  "v_accvgpr_write_b32 a102, v235\n\t" \
  "v_accvgpr_write_b32 a103, v236\n\t" \
  "v_accvgpr_read_b32 v233, a120\n\t" \
  "v_accvgpr_read_b32 v234, a121\n\t" \
  "v_accvgpr_read_b32 v235, a122\n\t" \
  "v_accvgpr_read_b32 v236, a123\n\t" \
  "v_mul_f32 v233, v233, v240\n\t" \
  "v_mul_f32 v234, v234, v240\n\t" \
  "v_mul_f32 v235, v235, v240\n\t" \
  "v_mul_f32 v236, v236, v240\n\t" \
  "v_accvgpr_write_b32 a120, v233\n\t" \
  "v_accvgpr_write_b32 a121, v234\n\t" \
  "v_accvgpr_write_b32 a122, v235\n\t" \
  "v_accvgpr_write_b32 a123, v236\n\t" \
  "v_accvgpr_read_b32 v233, a140\n\t" \
  "v_accvgpr_read_b32 v234, a141\n\t" \
  "v_accvgpr_read_b32 v235, a142\n\t" \
  "v_accvgpr_read_b32 v236, a143\n\t" \
  "v_mul_f32 v233, v233, v240\n\t" \
  "v_mul_f32 v234, v234, v240\n\t" \
  "v_mul_f32 v235, v235, v240\n\t" \
  "v_mul_f32 v236, v236, v240\n\t" \
  "v_accvgpr_write_b32 a140, v233\n\t" \
  "v_accvgpr_write_b32 a141, v234\n\t" \
  "v_accvgpr_write_b32 a142, v235\n\t" \
  "v_accvgpr_write_b32 a143, v236\n\t" \
  "v_accvgpr_read_b32 v233, a164\n\t" \
  "v_accvgpr_read_b32 v234, a165\n\t" \
  "v_accvgpr_read_b32 v235, a166\n\t" \
  "v_accvgpr_read_b32 v236, a167\n\t" \
  "v_mul_f32 v233, v233, v241\n\t" \
  "v_mul_f32 v234, v234, v241\n\t" \
  "v_mul_f32 v235, v235, v241\n\t" \
  "v_mul_f32 v236, v236, v241\n\t" \
  "v_accvgpr_write_b32 a164, v233\n\t" \
  "v_accvgpr_write_b32 a165, v234\n\t" \
  "v_accvgpr_write_b32 a166, v235\n\t" \
  "v_accvgpr_write_b32 a167, v236\n\t" \
  "v_accvgpr_read_b32 v233, a4\n\t" \
  "v_accvgpr_read_b32 v234, a5\n\t" \
  "v_accvgpr_read_b32 v235, a6\n\t" \
  "v_accvgpr_read_b32 v236, a7\n\t" \
  "v_mul_f32 v233, v233, v241\n\t" \
  "v_mul_f32 v234, v234, v241\n\t" \
  "v_mul_f32 v235, v235, v241\n\t" \
  "v_mul_f32 v236, v236, v241\n\t" \
  "v_accvgpr_write_b32 a4, v233\n\t" \
  "v_accvgpr_write_b32 a5, v234\n\t" \
  "v_accvgpr_write_b32 a6, v235\n\t" \
  "v_accvgpr_write_b32 a7, v236\n\t" \
  "v_accvgpr_read_b32 v233, a24\n\t" \
  "v_accvgpr_read_b32 v234, a25\n\t" \
  "v_accvgpr_read_b32 v235, a26\n\t" \
  "v_accvgpr_read_b32 v236, a27\n\t" \
  "v_mul_f32 v233, v233, v241\n\t" \
  "v_mul_f32 v234, v234, v241\n\t" \
  "v_mul_f32 v235, v235, v241\n\t" \
  "v_mul_f32 v236, v236, v241\n\t" \
  "v_accvgpr_write_b32 a24, v233\n\t" \
  "v_accvgpr_write_b32 a25, v234\n\t" \
  "v_accvgpr_write_b32 a26, v235\n\t" \
  "v_accvgpr_write_b32 a27, v236\n\t" \
  "v_accvgpr_read_b32 v233, a44\n\t" \
  "v_accvgpr_read_b32 v234, a45\n\t" \
  "v_accvgpr_read_b32 v235, a46\n\t" \
  "v_accvgpr_read_b32 v236, a47\n\t" \
  "v_mul_f32 v233, v233, v241\n\t" \
  "v_mul_f32 v234, v234, v241\n\t" \
  "v_mul_f32 v235, v235, v241\n\t" \
  "v_mul_f32 v236, v236, v241\n\t" \
  "v_accvgpr_write_b32 a44, v233\n\t" \
  "v_accvgpr_write_b32 a45, v234\n\t" \
  "v_accvgpr_write_b32 a46, v235\n\t" \
  "v_accvgpr_write_b32 a47, v236\n\t" \
  "v_accvgpr_read_b32 v233, a64\n\t" \
  "v_accvgpr_read_b32 v234, a65\n\t" \
  "v_accvgpr_read_b32 v235, a66\n\t" \
  "v_accvgpr_read_b32 v236, a67\n\t" \
  "v_mul_f32 v233, v233, v241\n\t" \
  "v_mul_f32 v234, v234, v241\n\t" \
  "v_mul_f32 v235, v235, v241\n\t" \
  "v_mul_f32 v236, v236, v241\n\t" \
  "v_accvgpr_write_b32 a64, v233\n\t" \
  "v_accvgpr_write_b32 a65, v234\n\t" \
  "v_accvgpr_write_b32 a66, v235\n\t" \
  "v_accvgpr_write_b32 a67, v236\n\t" \
  "v_accvgpr_read_b32 v233, a84\n\t" \
  "v_accvgpr_read_b32 v234, a85\n\t" \
  "v_accvgpr_read_b32 v235, a86\n\t" \
  "v_accvgpr_read_b32 v236, a87\n\t" \
  "v_mul_f32 v233, v233, v241\n\t" \
  "v_mul_f32 v234, v234, v241\n\t" \
  "v_mul_f32 v235, v235, v241\n\t" \
  "v_mul_f32 v236, v236, v241\n\t" \
  "v_accvgpr_write_b32 a84, v233\n\t" \
  "v_accvgpr_write_b32 a85, v234\n\t" \
  "v_accvgpr_write_b32 a86, v235\n\t" \
  "v_accvgpr_write_b32 a87, v236\n\t" \
  "v_accvgpr_read_b32 v233, a104\n\t" \
  "v_accvgpr_read_b32 v234, a105\n\t" \
  "v_accvgpr_read_b32 v235, a106\n\t" \
  "v_accvgpr_read_b32 v236, a107\n\t" \
  "v_mul_f32 v233, v233, v241\n\t" \
  "v_mul_f32 v234, v234, v241\n\t" \
  "v_mul_f32 v235, v235, v241\n\t" \
  "v_mul_f32 v236, v236, v241\n\t" \
  "v_accvgpr_write_b32 a104, v233\n\t" \
  "v_accvgpr_write_b32 a105, v234\n\t" \
  "v_accvgpr_write_b32 a106, v235\n\t" \
  "v_accvgpr_write_b32 a107, v236\n\t" \
  "v_accvgpr_read_b32 v233, a124\n\t" \
  "v_accvgpr_read_b32 v234, a125\n\t" \
  "v_accvgpr_read_b32 v235, a126\n\t" \
  "v_accvgpr_read_b32 v236, a127\n\t" \
  "v_mul_f32 v233, v233, v241\n\t" \
  "v_mul_f32 v234, v234, v241\n\t" \
  "v_mul_f32 v235, v235, v241\n\t" \
  "v_mul_f32 v236, v236, v241\n\t" \
  "v_accvgpr_write_b32 a124, v233\n\t" \
  "v_accvgpr_write_b32 a125, v234\n\t" \
  "v_accvgpr_write_b32 a126, v235\n\t" \
  "v_accvgpr_write_b32 a127, v236\n\t" \
  "v_accvgpr_read_b32 v233, a144\n\t" \
  "v_accvgpr_read_b32 v234, a145\n\t" \
  "v_accvgpr_read_b32 v235, a146\n\t" \
  "v_accvgpr_read_b32 v236, a147\n\t" \
  "v_mul_f32 v233, v233, v241\n\t" \
  "v_mul_f32 v234, v234, v241\n\t" \
  "v_mul_f32 v235, v235, v241\n\t" \
  "v_mul_f32 v236, v236, v241\n\t" \
  "v_accvgpr_write_b32 a144, v233\n\t" \
  "v_accvgpr_write_b32 a145, v234\n\t" \
  "v_accvgpr_write_b32 a146, v235\n\t" \
  "v_accvgpr_write_b32 a147, v236\n\t" \
  "v_accvgpr_read_b32 v233, a168\n\t" \
  "v_accvgpr_read_b32 v234, a169\n\t" \
  "v_accvgpr_read_b32 v235, a170\n\t" \
  "v_accvgpr_read_b32 v236, a171\n\t" \
  "v_mul_f32 v233, v233, v242\n\t" \
  "v_mul_f32 v234, v234, v242\n\t" \
  "v_mul_f32 v235, v235, v242\n\t" \
  "v_mul_f32 v236, v236, v242\n\t" \
  "v_accvgpr_write_b32 a168, v233\n\t" \
  "v_accvgpr_write_b32 a169, v234\n\t" \
  "v_accvgpr_write_b32 a170, v235\n\t" \
  "v_accvgpr_write_b32 a171, v236\n\t" \
  "v_accvgpr_read_b32 v233, a8\n\t" \
  "v_accvgpr_read_b32 v234, a9\n\t" \
  "v_accvgpr_read_b32 v235, a10\n\t" \
  "v_accvgpr_read_b32 v236, a11\n\t" \
  "v_mul_f32 v233, v233, v242\n\t" \
  "v_mul_f32 v234, v234, v242\n\t" \
  "v_mul_f32 v235, v235, v242\n\t" \
  "v_mul_f32 v236, v236, v242\n\t" \
  "v_accvgpr_write_b32 a8, v233\n\t" \
  "v_accvgpr_write_b32 a9, v234\n\t" \
  "v_accvgpr_write_b32 a10, v235\n\t" \
  "v_accvgpr_write_b32 a11, v236\n\t" \
  "v_accvgpr_read_b32 v233, a28\n\t" \
  "v_accvgpr_read_b32 v234, a29\n\t" \
  "v_accvgpr_read_b32 v235, a30\n\t" \
  "v_accvgpr_read_b32 v236, a31\n\t" \
  "v_mul_f32 v233, v233, v242\n\t" \
  "v_mul_f32 v234, v234, v242\n\t" \
  "v_mul_f32 v235, v235, v242\n\t" \
  "v_mul_f32 v236, v236, v242\n\t" \
  "v_accvgpr_write_b32 a28, v233\n\t" \
  "v_accvgpr_write_b32 a29, v234\n\t" \
  "v_accvgpr_write_b32 a30, v235\n\t" \
  "v_accvgpr_write_b32 a31, v236\n\t" \
  "v_accvgpr_read_b32 v233, a48\n\t" \
  "v_accvgpr_read_b32 v234, a49\n\t" \
  "v_accvgpr_read_b32 v235, a50\n\t" \
  "v_accvgpr_read_b32 v236, a51\n\t" \
  "v_mul_f32 v233, v233, v242\n\t" \
  "v_mul_f32 v234, v234, v242\n\t" \
  "v_mul_f32 v235, v235, v242\n\t" \
  "v_mul_f32 v236, v236, v242\n\t" \
  "v_accvgpr_write_b32 a48, v233\n\t" \
  "v_accvgpr_write_b32 a49, v234\n\t" \
  "v_accvgpr_write_b32 a50, v235\n\t" \
  "v_accvgpr_write_b32 a51, v236\n\t" \
  "v_accvgpr_read_b32 v233, a68\n\t" \
  "v_accvgpr_read_b32 v234, a69\n\t" \
  "v_accvgpr_read_b32 v235, a70\n\t" \
  "v_accvgpr_read_b32 v236, a71\n\t" \
  "v_mul_f32 v233, v233, v242\n\t" \
  "v_mul_f32 v234, v234, v242\n\t" \
  "v_mul_f32 v235, v235, v242\n\t" \
  "v_mul_f32 v236, v236, v242\n\t" \
  "v_accvgpr_write_b32 a68, v233\n\t" \
  "v_accvgpr_write_b32 a69, v234\n\t" \
  "v_accvgpr_write_b32 a70, v235\n\t" \
  "v_accvgpr_write_b32 a71, v236\n\t" \
  "v_accvgpr_read_b32 v233, a88\n\t" \
  "v_accvgpr_read_b32 v234, a89\n\t" \
  "v_accvgpr_read_b32 v235, a90\n\t" \
  "v_accvgpr_read_b32 v236, a91\n\t" \
  "v_mul_f32 v233, v233, v242\n\t" \
  "v_mul_f32 v234, v234, v242\n\t" \
  "v_mul_f32 v235, v235, v242\n\t" \
  "v_mul_f32 v236, v236, v242\n\t" \
  "v_accvgpr_write_b32 a88, v233\n\t" \
  "v_accvgpr_write_b32 a89, v234\n\t" \
  "v_accvgpr_write_b32 a90, v235\n\t" \
  "v_accvgpr_write_b32 a91, v236\n\t" \
  "v_accvgpr_read_b32 v233, a108\n\t" \
  "v_accvgpr_read_b32 v234, a109\n\t" \
  "v_accvgpr_read_b32 v235, a110\n\t" \
  "v_accvgpr_read_b32 v236, a111\n\t" \
  "v_mul_f32 v233, v233, v242\n\t" \
  "v_mul_f32 v234, v234, v242\n\t" \
  "v_mul_f32 v235, v235, v242\n\t" \
  "v_mul_f32 v236, v236, v242\n\t" \
  "v_accvgpr_write_b32 a108, v233\n\t" \
  "v_accvgpr_write_b32 a109, v234\n\t" \
  "v_accvgpr_write_b32 a110, v235\n\t" \
  "v_accvgpr_write_b32 a111, v236\n\t" \
  "v_accvgpr_read_b32 v233, a128\n\t" \
  "v_accvgpr_read_b32 v234, a129\n\t" \
  "v_accvgpr_read_b32 v235, a130\n\t" \
  "v_accvgpr_read_b32 v236, a131\n\t" \
  "v_mul_f32 v233, v233, v242\n\t" \
  "v_mul_f32 v234, v234, v242\n\t" \
  "v_mul_f32 v235, v235, v242\n\t" \
  "v_mul_f32 v236, v236, v242\n\t" \
  "v_accvgpr_write_b32 a128, v233\n\t" \
  "v_accvgpr_write_b32 a129, v234\n\t" \
  "v_accvgpr_write_b32 a130, v235\n\t" \
  "v_accvgpr_write_b32 a131, v236\n\t" \
  "v_accvgpr_read_b32 v233, a148\n\t" \
  "v_accvgpr_read_b32 v234, a149\n\t" \
  "v_accvgpr_read_b32 v235, a150\n\t" \
  "v_accvgpr_read_b32 v236, a151\n\t" \
  "v_mul_f32 v233, v233, v242\n\t" \
  "v_mul_f32 v234, v234, v242\n\t" \
  "v_mul_f32 v235, v235, v242\n\t" \
  "v_mul_f32 v236, v236, v242\n\t" \
  "v_accvgpr_write_b32 a148, v233\n\t" \
  "v_accvgpr_write_b32 a149, v234\n\t" \
  "v_accvgpr_write_b32 a150, v235\n\t" \
  "v_accvgpr_write_b32 a151, v236\n\t" \
  "v_accvgpr_read_b32 v233, a172\n\t" \
  "v_accvgpr_read_b32 v234, a173\n\t" \
  "v_accvgpr_read_b32 v235, a174\n\t" \
  "v_accvgpr_read_b32 v236, a175\n\t" \
  "v_mul_f32 v233, v233, v243\n\t" \
  "v_mul_f32 v234, v234, v243\n\t" \
  "v_mul_f32 v235, v235, v243\n\t" \
  "v_mul_f32 v236, v236, v243\n\t" \
  "v_accvgpr_write_b32 a172, v233\n\t" \
  "v_accvgpr_write_b32 a173, v234\n\t" \
  "v_accvgpr_write_b32 a174, v235\n\t" \
  "v_accvgpr_write_b32 a175, v236\n\t" \
  "v_accvgpr_read_b32 v233, a12\n\t" \
  "v_accvgpr_read_b32 v234, a13\n\t" \
  "v_accvgpr_read_b32 v235, a14\n\t" \
  "v_accvgpr_read_b32 v236, a15\n\t" \
  "v_mul_f32 v233, v233, v243\n\t" \
  "v_mul_f32 v234, v234, v243\n\t" \
  "v_mul_f32 v235, v235, v243\n\t" \
  "v_mul_f32 v236, v236, v243\n\t" \
  "v_accvgpr_write_b32 a12, v233\n\t" \
  "v_accvgpr_write_b32 a13, v234\n\t" \
  "v_accvgpr_write_b32 a14, v235\n\t" \
  "v_accvgpr_write_b32 a15, v236\n\t" \
  "v_accvgpr_read_b32 v233, a32\n\t" \
  "v_accvgpr_read_b32 v234, a33\n\t" \
  "v_accvgpr_read_b32 v235, a34\n\t" \
  "v_accvgpr_read_b32 v236, a35\n\t" \
  "v_mul_f32 v233, v233, v243\n\t" \
  "v_mul_f32 v234, v234, v243\n\t" \
  "v_mul_f32 v235, v235, v243\n\t" \
  "v_mul_f32 v236, v236, v243\n\t" \
  "v_accvgpr_write_b32 a32, v233\n\t" \
  "v_accvgpr_write_b32 a33, v234\n\t" \
  "v_accvgpr_write_b32 a34, v235\n\t" \
  "v_accvgpr_write_b32 a35, v236\n\t" \
  "v_accvgpr_read_b32 v233, a52\n\t" \
  "v_accvgpr_read_b32 v234, a53\n\t" \
  "v_accvgpr_read_b32 v235, a54\n\t" \
  "v_accvgpr_read_b32 v236, a55\n\t" \
  "v_mul_f32 v233, v233, v243\n\t" \
  "v_mul_f32 v234, v234, v243\n\t" \
  "v_mul_f32 v235, v235, v243\n\t" \
  "v_mul_f32 v236, v236, v243\n\t" \
  "v_accvgpr_write_b32 a52, v233\n\t" \
  "v_accvgpr_write_b32 a53, v234\n\t" \
  "v_accvgpr_write_b32 a54, v235\n\t" \
  "v_accvgpr_write_b32 a55, v236\n\t" \
  "v_accvgpr_read_b32 v233, a72\n\t" \
  "v_accvgpr_read_b32 v234, a73\n\t" \
  "v_accvgpr_read_b32 v235, a74\n\t" \
  "v_accvgpr_read_b32 v236, a75\n\t" \
  "v_mul_f32 v233, v233, v243\n\t" \
  "v_mul_f32 v234, v234, v243\n\t" \
  "v_mul_f32 v235, v235, v243\n\t" \
  "v_mul_f32 v236, v236, v243\n\t" \
  "v_accvgpr_write_b32 a72, v233\n\t" \
  "v_accvgpr_write_b32 a73, v234\n\t" \
  "v_accvgpr_write_b32 a74, v235\n\t" \
  "v_accvgpr_write_b32 a75, v236\n\t" \
  "v_accvgpr_read_b32 v233, a92\n\t" \
  "v_accvgpr_read_b32 v234, a93\n\t" \
  "v_accvgpr_read_b32 v235, a94\n\t" \
  "v_accvgpr_read_b32 v236, a95\n\t" \
  "v_mul_f32 v233, v233, v243\n\t" \
  "v_mul_f32 v234, v234, v243\n\t" \
  "v_mul_f32 v235, v235, v243\n\t" \
  "v_mul_f32 v236, v236, v243\n\t" \
  "v_accvgpr_write_b32 a92, v233\n\t" \
  "v_accvgpr_write_b32 a93, v234\n\t" \
  "v_accvgpr_write_b32 a94, v235\n\t" \
  "v_accvgpr_write_b32 a95, v236\n\t" \
  "v_accvgpr_read_b32 v233, a112\n\t" \
  "v_accvgpr_read_b32 v234, a113\n\t" \
  "v_accvgpr_read_b32 v235, a114\n\t" \
  "v_accvgpr_read_b32 v236, a115\n\t" \
  "v_mul_f32 v233, v233, v243\n\t" \
  "v_mul_f32 v234, v234, v243\n\t" \
  "v_mul_f32 v235, v235, v243\n\t" \
  "v_mul_f32 v236, v236, v243\n\t" \
  "v_accvgpr_write_b32 a112, v233\n\t" \
  "v_accvgpr_write_b32 a113, v234\n\t" \
  "v_accvgpr_write_b32 a114, v235\n\t" \
  "v_accvgpr_write_b32 a115, v236\n\t" \
  "v_accvgpr_read_b32 v233, a132\n\t" \
  "v_accvgpr_read_b32 v234, a133\n\t" \
  "v_accvgpr_read_b32 v235, a134\n\t" \
  "v_accvgpr_read_b32 v236, a135\n\t" \
  "v_mul_f32 v233, v233, v243\n\t" \
  "v_mul_f32 v234, v234, v243\n\t" \
  "v_mul_f32 v235, v235, v243\n\t" \
  "v_mul_f32 v236, v236, v243\n\t" \
  "v_accvgpr_write_b32 a132, v233\n\t" \
  "v_accvgpr_write_b32 a133, v234\n\t" \
  "v_accvgpr_write_b32 a134, v235\n\t" \
  "v_accvgpr_write_b32 a135, v236\n\t" \
  "v_accvgpr_read_b32 v233, a152\n\t" \
  "v_accvgpr_read_b32 v234, a153\n\t" \
  "v_accvgpr_read_b32 v235, a154\n\t" \
  "v_accvgpr_read_b32 v236, a155\n\t" \
  "v_mul_f32 v233, v233, v243\n\t" \
  "v_mul_f32 v234, v234, v243\n\t" \
  "v_mul_f32 v235, v235, v243\n\t" \
  "v_mul_f32 v236, v236, v243\n\t" \
  "v_accvgpr_write_b32 a152, v233\n\t" \
  "v_accvgpr_write_b32 a153, v234\n\t" \
  "v_accvgpr_write_b32 a154, v235\n\t" \
  "v_accvgpr_write_b32 a155, v236\n\t" \
  "v_accvgpr_read_b32 v233, a176\n\t" \
  "v_accvgpr_read_b32 v234, a177\n\t" \
  "v_accvgpr_read_b32 v235, a178\n\t" \
  "v_accvgpr_read_b32 v236, a179\n\t" \
  "v_mul_f32 v233, v233, v244\n\t" \
  "v_mul_f32 v234, v234, v244\n\t" \
  "v_mul_f32 v235, v235, v244\n\t" \
  "v_mul_f32 v236, v236, v244\n\t" \
  "v_accvgpr_write_b32 a176, v233\n\t" \
  "v_accvgpr_write_b32 a177, v234\n\t" \
  "v_accvgpr_write_b32 a178, v235\n\t" \
  "v_accvgpr_write_b32 a179, v236\n\t" \
  "v_accvgpr_read_b32 v233, a16\n\t" \
  "v_accvgpr_read_b32 v234, a17\n\t" \
  "v_accvgpr_read_b32 v235, a18\n\t" \
  "v_accvgpr_read_b32 v236, a19\n\t" \
  "v_mul_f32 v233, v233, v244\n\t" \
  "v_mul_f32 v234, v234, v244\n\t" \
  "v_mul_f32 v235, v235, v244\n\t" \
  "v_mul_f32 v236, v236, v244\n\t" \
  "v_accvgpr_write_b32 a16, v233\n\t" \
  "v_accvgpr_write_b32 a17, v234\n\t" \
  "v_accvgpr_write_b32 a18, v235\n\t" \
  "v_accvgpr_write_b32 a19, v236\n\t" \
  "v_accvgpr_read_b32 v233, a36\n\t" \
  "v_accvgpr_read_b32 v234, a37\n\t" \
  "v_accvgpr_read_b32 v235, a38\n\t" \
  "v_accvgpr_read_b32 v236, a39\n\t" \
  "v_mul_f32 v233, v233, v244\n\t" \
  "v_mul_f32 v234, v234, v244\n\t" \
  "v_mul_f32 v235, v235, v244\n\t" \
  "v_mul_f32 v236, v236, v244\n\t" \
  "v_accvgpr_write_b32 a36, v233\n\t" \
  "v_accvgpr_write_b32 a37, v234\n\t" \
  "v_accvgpr_write_b32 a38, v235\n\t" \
  "v_accvgpr_write_b32 a39, v236\n\t" \
  "v_accvgpr_read_b32 v233, a56\n\t" \
  "v_accvgpr_read_b32 v234, a57\n\t" \
  "v_accvgpr_read_b32 v235, a58\n\t" \
  "v_accvgpr_read_b32 v236, a59\n\t" \
  "v_mul_f32 v233, v233, v244\n\t" \
  "v_mul_f32 v234, v234, v244\n\t" \
  "v_mul_f32 v235, v235, v244\n\t" \
  "v_mul_f32 v236, v236, v244\n\t" \
  "v_accvgpr_write_b32 a56, v233\n\t" \
  "v_accvgpr_write_b32 a57, v234\n\t" \
  "v_accvgpr_write_b32 a58, v235\n\t" \
  "v_accvgpr_write_b32 a59, v236\n\t" \
  "v_accvgpr_read_b32 v233, a76\n\t" \
  "v_accvgpr_read_b32 v234, a77\n\t" \
  "v_accvgpr_read_b32 v235, a78\n\t" \
  "v_accvgpr_read_b32 v236, a79\n\t" \
  "v_mul_f32 v233, v233, v244\n\t" \
  "v_mul_f32 v234, v234, v244\n\t" \
  "v_mul_f32 v235, v235, v244\n\t" \
  "v_mul_f32 v236, v236, v244\n\t" \
  "v_accvgpr_write_b32 a76, v233\n\t" \
  "v_accvgpr_write_b32 a77, v234\n\t" \
  "v_accvgpr_write_b32 a78, v235\n\t" \
  "v_accvgpr_write_b32 a79, v236\n\t" \
  "v_accvgpr_read_b32 v233, a96\n\t" \
  "v_accvgpr_read_b32 v234, a97\n\t" \
  "v_accvgpr_read_b32 v235, a98\n\t" \
  "v_accvgpr_read_b32 v236, a99\n\t" \
  "v_mul_f32 v233, v233, v244\n\t" \
  "v_mul_f32 v234, v234, v244\n\t" \
  "v_mul_f32 v235, v235, v244\n\t" \
  "v_mul_f32 v236, v236, v244\n\t" \
  "v_accvgpr_write_b32 a96, v233\n\t" \
  "v_accvgpr_write_b32 a97, v234\n\t" \
  "v_accvgpr_write_b32 a98, v235\n\t" \
  "v_accvgpr_write_b32 a99, v236\n\t" \
  "v_accvgpr_read_b32 v233, a116\n\t" \
  "v_accvgpr_read_b32 v234, a117\n\t" \
  "v_accvgpr_read_b32 v235, a118\n\t" \
  "v_accvgpr_read_b32 v236, a119\n\t" \
  "v_mul_f32 v233, v233, v244\n\t" \
  "v_mul_f32 v234, v234, v244\n\t" \
  "v_mul_f32 v235, v235, v244\n\t" \
  "v_mul_f32 v236, v236, v244\n\t" \
  "v_accvgpr_write_b32 a116, v233\n\t" \
  "v_accvgpr_write_b32 a117, v234\n\t" \
  "v_accvgpr_write_b32 a118, v235\n\t" \
  "v_accvgpr_write_b32 a119, v236\n\t" \
  "v_accvgpr_read_b32 v233, a136\n\t" \
  "v_accvgpr_read_b32 v234, a137\n\t" \
  "v_accvgpr_read_b32 v235, a138\n\t" \
  "v_accvgpr_read_b32 v236, a139\n\t" \
  "v_mul_f32 v233, v233, v244\n\t" \
  "v_mul_f32 v234, v234, v244\n\t" \
  "v_mul_f32 v235, v235, v244\n\t" \
  "v_mul_f32 v236, v236, v244\n\t" \
  "v_accvgpr_write_b32 a136, v233\n\t" \
  "v_accvgpr_write_b32 a137, v234\n\t" \
  "v_accvgpr_write_b32 a138, v235\n\t" \
  "v_accvgpr_write_b32 a139, v236\n\t" \
  "v_accvgpr_read_b32 v233, a156\n\t" \
  "v_accvgpr_read_b32 v234, a157\n\t" \
  "v_accvgpr_read_b32 v235, a158\n\t" \
  "v_accvgpr_read_b32 v236, a159\n\t" \
  "v_mul_f32 v233, v233, v244\n\t" \
  "v_mul_f32 v234, v234, v244\n\t" \
  "v_mul_f32 v235, v235, v244\n\t" \
  "v_mul_f32 v236, v236, v244\n\t" \
  "v_accvgpr_write_b32 a156, v233\n\t" \
  "v_accvgpr_write_b32 a157, v234\n\t" \
  "v_accvgpr_write_b32 a158, v235\n\t" \
  "v_accvgpr_write_b32 a159, v236\n\t" \
  "s_mov_b32 s73, 0\n\t" \
  "s_add_u32 s60, s60, 4\n\t" \
  "s_addc_u32 s61, s61, 0\n\t" \
  "s_setpc_b64 s[60:61]\n\t" \
  ".Lend_%=:\n\t"

#define ATTN_CLOB "a0","a1","a2","a3","a4","a5","a6","a7","a8","a9","a10","a11","a12","a13","a14","a15","a16","a17","a18","a19","a20","a21","a22","a23","a24","a25","a26","a27","a28","a29","a30","a31","a32","a33","a34","a35","a36","a37","a38","a39","a40","a41","a42","a43","a44","a45","a46","a47","a48","a49","a50","a51","a52","a53","a54","a55","a56","a57","a58","a59","a60","a61","a62","a63","a64","a65","a66","a67","a68","a69","a70","a71","a72","a73","a74","a75","a76","a77","a78","a79","a80","a81","a82","a83","a84","a85","a86","a87","a88","a89","a90","a91","a92","a93","a94","a95","a96","a97","a98","a99","a100","a101","a102","a103","a104","a105","a106","a107","a108","a109","a110","a111","a112","a113","a114","a115","a116","a117","a118","a119","a120","a121","a122","a123","a124","a125","a126","a127","a128","a129","a130","a131","a132","a133","a134","a135","a136","a137","a138","a139","a140","a141","a142","a143","a144","a145","a146","a147","a148","a149","a150","a151","a152","a153","a154","a155","a156","a157","a158","a159","a160","a161","a162","a163","a164","a165","a166","a167","a168","a169","a170","a171","a172","a173","a174","a175","a176","a177","a178","a179","a180","a181","a182","a183","a184","a185","a186","a187","a188","a189","a190","a191","a192","a193","a194","a195","a196","a197","a198","a199","a200","a201","a202","a203","a204","a205","a206","a207","a208","a209","a210","a211","a212","a213","a214","a215","a216","a217","a218","a219","a220","a221","a222","a223","a224","a225","a226","a227","a228","a229","a230","a231","a232","a233","a234","a235","a236","a237","a238","a239","a240","a241","a242","a243","a244","a245","a246","a247","a248","a249","a250","a251","a252","a253","a254","a255","v56","v57","v58","v59","v60","v61","v62","v63","v64","v65","v66","v67","v68","v69","v70","v71","v72","v73","v74","v75","v76","v77","v78","v79","v80","v81","v82","v83","v84","v85","v86","v87","v88","v89","v90","v91","v92","v93","v94","v95","v96","v97","v98","v99","v100","v101","v102","v103","v104","v105","v106","v107","v108","v109","v110","v111","v112","v113","v114","v115","v116","v117","v118","v119","v120","v121","v122","v123","v124","v125","v126","v127","v128","v129","v130","v131","v132","v133","v134","v135","v136","v137","v138","v139","v140","v141","v142","v143","v144","v145","v146","v147","v148","v149","v150","v151","v152","v153","v154","v155","v156","v157","v158","v159","v160","v161","v162","v163","v164","v165","v166","v167","v168","v169","v170","v171","v172","v173","v174","v175","v176","v177","v178","v179","v180","v181","v182","v183","v184","v185","v186","v187","v188","v189","v190","v191","v192","v193","v194","v195","v196","v197","v198","v199","v200","v201","v202","v203","v204","v205","v206","v207","v208","v209","v210","v211","v212","v213","v214","v215","v216","v217","v218","v219","v220","v221","v222","v223","v224","v225","v226","v227","v228","v229","v230","v231","v232","v233","v234","v235","v236","v237","v238","v239","v240","v241","v242","v243","v244","v245","v246","v247","v248","v249","v250","v251","v252","v253","v254","v255","s56","s57","s58","s59","s60","s61","s62","s63","s64","s65","s66","s67","s68","s69","s70","s71","s72","s73","s74","s75","s76","s77","s78","s79","s80","s81","s82","s83","s84","s85","s86","s87","s88","s89","s90","s91","s92","s93","s94","s95","s96","s97","s98","s99"
#define RED_ASM \
  "s_mov_b32 s56, 0\n\t" \
  "s_mov_b32 s57, 0\n\t" \
  "s_mov_b32 s61, 0\n\t" \
  "s_mov_b32 s60, 0x20000\n\t" \
  ".LRpoll_%=:\n\t" \
  "global_load_dwordx2 v[60:61], %[fp], off sc1\n\t" \
  "s_waitcnt vmcnt(0)\n\t" \
  "v_cmp_eq_u64 vcc, v[60:61], %[tag]\n\t" \
  "s_andn2_b32 s59, s56, s57\n\t" \
  "s_mov_b32 s57, s56\n\t" \
  "s_andn2_b32 s62, vcc_lo, s61\n\t" \
  "s_cbranch_scc0 .LRnone_%=\n\t" \
  "s_mov_b32 s61, vcc_lo\n\t" \
  "s_bfe_u32 s58, s62, 0x40000\n\t" \
  "s_cbranch_scc0 .LRi0_%=\n\t" \
  "s_bitcmp1_b32 s62, 0\n\t" \
  "s_cbranch_scc0 .LRp0_%=\n\t" \
  "s_add_u32 s58, %[so], 0\n\t" \
  "buffer_load_dwordx4 v[64:67], %[l16], %[srdR], s58 offen sc1\n\t" \
  ".LRp0_%=:\n\t" \
  "s_bitcmp1_b32 s62, 1\n\t" \
  "s_cbranch_scc0 .LRp1_%=\n\t" \
  "s_add_u32 s58, %[so], 81920\n\t" \
  "buffer_load_dwordx4 v[68:71], %[l16], %[srdR], s58 offen sc1\n\t" \
  ".LRp1_%=:\n\t" \
  "s_bitcmp1_b32 s62, 2\n\t" \
  "s_cbranch_scc0 .LRp2_%=\n\t" \
  "s_add_u32 s58, %[so], 163840\n\t" \
  "buffer_load_dwordx4 v[72:75], %[l16], %[srdR], s58 offen sc1\n\t" \
  ".LRp2_%=:\n\t" \
  "s_bitcmp1_b32 s62, 3\n\t" \
  "s_cbranch_scc0 .LRp3_%=\n\t" \
  "s_add_u32 s58, %[so], 245760\n\t" \
  "buffer_load_dwordx4 v[76:79], %[l16], %[srdR], s58 offen sc1\n\t" \
  ".LRp3_%=:\n\t" \
  "s_bfe_u32 s58, s61, 0x40000\n\t" \
  "s_cmp_eq_u32 s58, 15\n\t" \
  "s_cbranch_scc0 .LRi0_%=\n\t" \
  "s_add_u32 s58, %[se], 0\n\t" \
  "buffer_load_dwordx4 v[80:83], %[e16], %[srdR], s58 offen sc1\n\t" \
  "s_bitset1_b32 s56, 0\n\t" \
  ".LRi0_%=:\n\t" \
  "s_bfe_u32 s58, s62, 0x40004\n\t" \
  "s_cbranch_scc0 .LRi1_%=\n\t" \
  "s_bitcmp1_b32 s62, 4\n\t" \
  "s_cbranch_scc0 .LRp4_%=\n\t" \
  "s_add_u32 s58, %[so], 327680\n\t" \
  "buffer_load_dwordx4 v[84:87], %[l16], %[srdR], s58 offen sc1\n\t" \
  ".LRp4_%=:\n\t" \
  "s_bitcmp1_b32 s62, 5\n\t" \
  "s_cbranch_scc0 .LRp5_%=\n\t" \
  "s_add_u32 s58, %[so], 409600\n\t" \
  "buffer_load_dwordx4 v[88:91], %[l16], %[srdR], s58 offen sc1\n\t" \
  ".LRp5_%=:\n\t" \
  "s_bitcmp1_b32 s62, 6\n\t" \
  "s_cbranch_scc0 .LRp6_%=\n\t" \
  "s_add_u32 s58, %[so], 491520\n\t" \
  "buffer_load_dwordx4 v[92:95], %[l16], %[srdR], s58 offen sc1\n\t" \
  ".LRp6_%=:\n\t" \
  "s_bitcmp1_b32 s62, 7\n\t" \
  "s_cbranch_scc0 .LRp7_%=\n\t" \
  "s_add_u32 s58, %[so], 573440\n\t" \
  "buffer_load_dwordx4 v[96:99], %[l16], %[srdR], s58 offen sc1\n\t" \
  ".LRp7_%=:\n\t" \
  "s_bfe_u32 s58, s61, 0x40004\n\t" \
  "s_cmp_eq_u32 s58, 15\n\t" \
  "s_cbranch_scc0 .LRi1_%=\n\t" \
  "s_add_u32 s58, %[se], 327680\n\t" \
  "buffer_load_dwordx4 v[100:103], %[e16], %[srdR], s58 offen sc1\n\t" \
  "s_bitset1_b32 s56, 1\n\t" \
  ".LRi1_%=:\n\t" \
  "s_bfe_u32 s58, s62, 0x40008\n\t" \
  "s_cbranch_scc0 .LRi2_%=\n\t" \
  "s_bitcmp1_b32 s62, 8\n\t" \
  "s_cbranch_scc0 .LRp8_%=\n\t" \
  "s_add_u32 s58, %[so], 655360\n\t" \
  "buffer_load_dwordx4 v[104:107], %[l16], %[srdR], s58 offen sc1\n\t" \
  ".LRp8_%=:\n\t" \
  "s_bitcmp1_b32 s62, 9\n\t" \
  "s_cbranch_scc0 .LRp9_%=\n\t" \
  "s_add_u32 s58, %[so], 737280\n\t" \
  "buffer_load_dwordx4 v[108:111], %[l16], %[srdR], s58 offen sc1\n\t" \
  ".LRp9_%=:\n\t" \
  "s_bitcmp1_b32 s62, 10\n\t" \
  "s_cbranch_scc0 .LRp10_%=\n\t" \
  "s_add_u32 s58, %[so], 819200\n\t" \
  "buffer_load_dwordx4 v[112:115], %[l16], %[srdR], s58 offen sc1\n\t" \
  ".LRp10_%=:\n\t" \
  "s_bitcmp1_b32 s62, 11\n\t" \
  "s_cbranch_scc0 .LRp11_%=\n\t" \
  "s_add_u32 s58, %[so], 901120\n\t" \
  "buffer_load_dwordx4 v[116:119], %[l16], %[srdR], s58 offen sc1\n\t" \
  ".LRp11_%=:\n\t" \
  "s_bfe_u32 s58, s61, 0x40008\n\t" \
  "s_cmp_eq_u32 s58, 15\n\t" \
  "s_cbranch_scc0 .LRi2_%=\n\t" \
  "s_add_u32 s58, %[se], 655360\n\t" \
  "buffer_load_dwordx4 v[120:123], %[e16], %[srdR], s58 offen sc1\n\t" \
  "s_bitset1_b32 s56, 2\n\t" \
  ".LRi2_%=:\n\t" \
  "s_bfe_u32 s58, s62, 0x4000c\n\t" \
  "s_cbranch_scc0 .LRi3_%=\n\t" \
  "s_bitcmp1_b32 s62, 12\n\t" \
  "s_cbranch_scc0 .LRp12_%=\n\t" \
  "s_add_u32 s58, %[so], 983040\n\t" \
  "buffer_load_dwordx4 v[124:127], %[l16], %[srdR], s58 offen sc1\n\t" \
  ".LRp12_%=:\n\t" \
  "s_bitcmp1_b32 s62, 13\n\t" \
  "s_cbranch_scc0 .LRp13_%=\n\t" \
  "s_add_u32 s58, %[so], 1064960\n\t" \
  "buffer_load_dwordx4 v[128:131], %[l16], %[srdR], s58 offen sc1\n\t" \
  ".LRp13_%=:\n\t" \
  "s_bitcmp1_b32 s62, 14\n\t" \
  "s_cbranch_scc0 .LRp14_%=\n\t" \
  "s_add_u32 s58, %[so], 1146880\n\t" \
  "buffer_load_dwordx4 v[132:135], %[l16], %[srdR], s58 offen sc1\n\t" \
  ".LRp14_%=:\n\t" \
  "s_bitcmp1_b32 s62, 15\n\t" \
  "s_cbranch_scc0 .LRp15_%=\n\t" \
  "s_add_u32 s58, %[so], 1228800\n\t" \
  "buffer_load_dwordx4 v[136:139], %[l16], %[srdR], s58 offen sc1\n\t" \
  ".LRp15_%=:\n\t" \
  "s_bfe_u32 s58, s61, 0x4000c\n\t" \
  "s_cmp_eq_u32 s58, 15\n\t" \
  "s_cbranch_scc0 .LRi3_%=\n\t" \
  "s_add_u32 s58, %[se], 983040\n\t" \
  "buffer_load_dwordx4 v[140:143], %[e16], %[srdR], s58 offen sc1\n\t" \
  "s_bitset1_b32 s56, 3\n\t" \
  ".LRi3_%=:\n\t" \
  ".LRnone_%=:\n\t" \
  "s_bitcmp1_b32 s59, 0\n\t" \
  "s_cbranch_scc0 .LRa0_%=\n\t" \
  "v_mfma_f32_16x16x32_bf16 v[144:147], %[slo], v[64:67], 0\n\t" \
  "v_mfma_f32_16x16x32_bf16 v[148:151], %[shi], v[64:67], 0\n\t" \
  "v_mfma_f32_16x16x32_bf16 v[144:147], %[slo], v[68:71], v[144:147]\n\t" \
  "v_mfma_f32_16x16x32_bf16 v[148:151], %[shi], v[68:71], v[148:151]\n\t" \
  "v_mfma_f32_16x16x32_bf16 v[144:147], %[slo], v[72:75], v[144:147]\n\t" \
  "v_mfma_f32_16x16x32_bf16 v[148:151], %[shi], v[72:75], v[148:151]\n\t" \
  "v_mfma_f32_16x16x32_bf16 v[144:147], %[slo], v[76:79], v[144:147]\n\t" \
  "v_mfma_f32_16x16x32_bf16 v[148:151], %[shi], v[76:79], v[148:151]\n\t" \
  "v_mfma_f32_16x16x32_bf16 v[152:155], %[sfi], v[80:83], 0\n\t" \
  ".LRa0_%=:\n\t" \
  "s_bitcmp1_b32 s59, 1\n\t" \
  "s_cbranch_scc0 .LRa1_%=\n\t" \
  "v_mfma_f32_16x16x32_bf16 v[156:159], %[slo], v[84:87], 0\n\t" \
  "v_mfma_f32_16x16x32_bf16 v[160:163], %[shi], v[84:87], 0\n\t" \
  "v_mfma_f32_16x16x32_bf16 v[156:159], %[slo], v[88:91], v[156:159]\n\t" \
  "v_mfma_f32_16x16x32_bf16 v[160:163], %[shi], v[88:91], v[160:163]\n\t" \
  "v_mfma_f32_16x16x32_bf16 v[156:159], %[slo], v[92:95], v[156:159]\n\t" \
  "v_mfma_f32_16x16x32_bf16 v[160:163], %[shi], v[92:95], v[160:163]\n\t" \
  "v_mfma_f32_16x16x32_bf16 v[156:159], %[slo], v[96:99], v[156:159]\n\t" \
  "v_mfma_f32_16x16x32_bf16 v[160:163], %[shi], v[96:99], v[160:163]\n\t" \
  "v_mfma_f32_16x16x32_bf16 v[164:167], %[sfi], v[100:103], 0\n\t" \
  ".LRa1_%=:\n\t" \
  "s_bitcmp1_b32 s59, 2\n\t" \
  "s_cbranch_scc0 .LRa2_%=\n\t" \
  "v_mfma_f32_16x16x32_bf16 v[168:171], %[slo], v[104:107], 0\n\t" \
  "v_mfma_f32_16x16x32_bf16 v[172:175], %[shi], v[104:107], 0\n\t" \
  "v_mfma_f32_16x16x32_bf16 v[168:171], %[slo], v[108:111], v[168:171]\n\t" \
  "v_mfma_f32_16x16x32_bf16 v[172:175], %[shi], v[108:111], v[172:175]\n\t" \
  "v_mfma_f32_16x16x32_bf16 v[168:171], %[slo], v[112:115], v[168:171]\n\t" \
  "v_mfma_f32_16x16x32_bf16 v[172:175], %[shi], v[112:115], v[172:175]\n\t" \
  "v_mfma_f32_16x16x32_bf16 v[168:171], %[slo], v[116:119], v[168:171]\n\t" \
  "v_mfma_f32_16x16x32_bf16 v[172:175], %[shi], v[116:119], v[172:175]\n\t" \
  "v_mfma_f32_16x16x32_bf16 v[176:179], %[sfi], v[120:123], 0\n\t" \
  ".LRa2_%=:\n\t" \
  "s_bitcmp1_b32 s59, 3\n\t" \
  "s_cbranch_scc0 .LRa3_%=\n\t" \
  "v_mfma_f32_16x16x32_bf16 v[180:183], %[slo], v[124:127], 0\n\t" \
  "v_mfma_f32_16x16x32_bf16 v[184:187], %[shi], v[124:127], 0\n\t" \
  "v_mfma_f32_16x16x32_bf16 v[180:183], %[slo], v[128:131], v[180:183]\n\t" \
  "v_mfma_f32_16x16x32_bf16 v[184:187], %[shi], v[128:131], v[184:187]\n\t" \
  "v_mfma_f32_16x16x32_bf16 v[180:183], %[slo], v[132:135], v[180:183]\n\t" \
  "v_mfma_f32_16x16x32_bf16 v[184:187], %[shi], v[132:135], v[184:187]\n\t" \
  "v_mfma_f32_16x16x32_bf16 v[180:183], %[slo], v[136:139], v[180:183]\n\t" \
  "v_mfma_f32_16x16x32_bf16 v[184:187], %[shi], v[136:139], v[184:187]\n\t" \
  "v_mfma_f32_16x16x32_bf16 v[188:191], %[sfi], v[140:143], 0\n\t" \
  ".LRa3_%=:\n\t" \
  "s_cmp_eq_u32 s56, 15\n\t" \
  "s_cbranch_scc1 .LRdone_%=\n\t" \
  "s_sub_u32 s60, s60, 1\n\t" \
  "s_cbranch_scc1 .LRdone_%=\n\t" \
  "s_sleep 1\n\t" \
  "s_branch .LRpoll_%=\n\t" \
  ".LRdone_%=:\n\t" \
  "s_waitcnt vmcnt(0)\n\t" \
  "s_andn2_b32 s58, s56, s57\n\t" \
  "s_mov_b32 s57, s56\n\t" \
  "s_bitcmp1_b32 s58, 0\n\t" \
  "s_cbranch_scc0 .LRcf0_%=\n\t" \
  "v_mfma_f32_16x16x32_bf16 v[144:147], %[slo], v[64:67], 0\n\t" \
  "v_mfma_f32_16x16x32_bf16 v[148:151], %[shi], v[64:67], 0\n\t" \
  "v_mfma_f32_16x16x32_bf16 v[144:147], %[slo], v[68:71], v[144:147]\n\t" \
  "v_mfma_f32_16x16x32_bf16 v[148:151], %[shi], v[68:71], v[148:151]\n\t" \
  "v_mfma_f32_16x16x32_bf16 v[144:147], %[slo], v[72:75], v[144:147]\n\t" \
  "v_mfma_f32_16x16x32_bf16 v[148:151], %[shi], v[72:75], v[148:151]\n\t" \
  "v_mfma_f32_16x16x32_bf16 v[144:147], %[slo], v[76:79], v[144:147]\n\t" \
  "v_mfma_f32_16x16x32_bf16 v[148:151], %[shi], v[76:79], v[148:151]\n\t" \
  "v_mfma_f32_16x16x32_bf16 v[152:155], %[sfi], v[80:83], 0\n\t" \
  ".LRcf0_%=:\n\t" \
  "s_bitcmp1_b32 s58, 1\n\t" \
  "s_cbranch_scc0 .LRcf1_%=\n\t" \
  "v_mfma_f32_16x16x32_bf16 v[156:159], %[slo], v[84:87], 0\n\t" \
  "v_mfma_f32_16x16x32_bf16 v[160:163], %[shi], v[84:87], 0\n\t" \
  "v_mfma_f32_16x16x32_bf16 v[156:159], %[slo], v[88:91], v[156:159]\n\t" \
  "v_mfma_f32_16x16x32_bf16 v[160:163], %[shi], v[88:91], v[160:163]\n\t" \
  "v_mfma_f32_16x16x32_bf16 v[156:159], %[slo], v[92:95], v[156:159]\n\t" \
  "v_mfma_f32_16x16x32_bf16 v[160:163], %[shi], v[92:95], v[160:163]\n\t" \
  "v_mfma_f32_16x16x32_bf16 v[156:159], %[slo], v[96:99], v[156:159]\n\t" \
  "v_mfma_f32_16x16x32_bf16 v[160:163], %[shi], v[96:99], v[160:163]\n\t" \
  "v_mfma_f32_16x16x32_bf16 v[164:167], %[sfi], v[100:103], 0\n\t" \
  ".LRcf1_%=:\n\t" \
  "s_bitcmp1_b32 s58, 2\n\t" \
  "s_cbranch_scc0 .LRcf2_%=\n\t" \
  "v_mfma_f32_16x16x32_bf16 v[168:171], %[slo], v[104:107], 0\n\t" \
  "v_mfma_f32_16x16x32_bf16 v[172:175], %[shi], v[104:107], 0\n\t" \
  "v_mfma_f32_16x16x32_bf16 v[168:171], %[slo], v[108:111], v[168:171]\n\t" \
  "v_mfma_f32_16x16x32_bf16 v[172:175], %[shi], v[108:111], v[172:175]\n\t" \
  "v_mfma_f32_16x16x32_bf16 v[168:171], %[slo], v[112:115], v[168:171]\n\t" \
  "v_mfma_f32_16x16x32_bf16 v[172:175], %[shi], v[112:115], v[172:175]\n\t" \
  "v_mfma_f32_16x16x32_bf16 v[168:171], %[slo], v[116:119], v[168:171]\n\t" \
  "v_mfma_f32_16x16x32_bf16 v[172:175], %[shi], v[116:119], v[172:175]\n\t" \
  "v_mfma_f32_16x16x32_bf16 v[176:179], %[sfi], v[120:123], 0\n\t" \
  ".LRcf2_%=:\n\t" \
  "s_bitcmp1_b32 s58, 3\n\t" \
  "s_cbranch_scc0 .LRcf3_%=\n\t" \
  "v_mfma_f32_16x16x32_bf16 v[180:183], %[slo], v[124:127], 0\n\t" \
  "v_mfma_f32_16x16x32_bf16 v[184:187], %[shi], v[124:127], 0\n\t" \
  "v_mfma_f32_16x16x32_bf16 v[180:183], %[slo], v[128:131], v[180:183]\n\t" \
  "v_mfma_f32_16x16x32_bf16 v[184:187], %[shi], v[128:131], v[184:187]\n\t" \
  "v_mfma_f32_16x16x32_bf16 v[180:183], %[slo], v[132:135], v[180:183]\n\t" \
  "v_mfma_f32_16x16x32_bf16 v[184:187], %[shi], v[132:135], v[184:187]\n\t" \
  "v_mfma_f32_16x16x32_bf16 v[180:183], %[slo], v[136:139], v[180:183]\n\t" \
  "v_mfma_f32_16x16x32_bf16 v[184:187], %[shi], v[136:139], v[184:187]\n\t" \
  "v_mfma_f32_16x16x32_bf16 v[188:191], %[sfi], v[140:143], 0\n\t" \
  ".LRcf3_%=:\n\t" \
  "s_nop 7\n\t" \
  "v_pk_add_f32 v[144:145], v[144:145], v[156:157]\n\t" \
  "v_pk_add_f32 v[168:169], v[168:169], v[180:181]\n\t" \
  "v_pk_add_f32 v[146:147], v[146:147], v[158:159]\n\t" \
  "v_pk_add_f32 v[170:171], v[170:171], v[182:183]\n\t" \
  "v_pk_add_f32 v[148:149], v[148:149], v[160:161]\n\t" \
  "v_pk_add_f32 v[172:173], v[172:173], v[184:185]\n\t" \
  "v_pk_add_f32 v[150:151], v[150:151], v[162:163]\n\t" \
  "v_pk_add_f32 v[174:175], v[174:175], v[186:187]\n\t" \
  "v_pk_add_f32 v[152:153], v[152:153], v[164:165]\n\t" \
  "v_pk_add_f32 v[176:177], v[176:177], v[188:189]\n\t" \
  "v_pk_add_f32 v[154:155], v[154:155], v[166:167]\n\t" \
  "v_pk_add_f32 v[178:179], v[178:179], v[190:191]\n\t" \
  "v_pk_add_f32 v[144:145], v[144:145], v[168:169]\n\t" \
  "v_pk_add_f32 v[146:147], v[146:147], v[170:171]\n\t" \
  "v_pk_add_f32 v[148:149], v[148:149], v[172:173]\n\t" \
  "v_pk_add_f32 v[150:151], v[150:151], v[174:175]\n\t" \
  "v_pk_add_f32 v[152:153], v[152:153], v[176:177]\n\t" \
  "v_pk_add_f32 v[154:155], v[154:155], v[178:179]\n\t" \
  "v_cvt_pk_bf16_f32 v64, v144, v145\n\t" \
  "v_cvt_pk_bf16_f32 v65, v146, v147\n\t" \
  "v_cvt_pk_bf16_f32 v66, v148, v149\n\t" \
  "v_cvt_pk_bf16_f32 v67, v150, v151\n\t" \
  "v_cvt_pk_bf16_f32 v68, v152, v153\n\t" \
  "v_cvt_pk_bf16_f32 v69, v154, v155\n\t" \
  "buffer_store_dwordx2 v[64:65], %[oo0], %[srdO], 0 offen\n\t" \
  "buffer_store_dwordx2 v[66:67], %[oo0], %[srdO], 0 offen offset:32\n\t" \
  "buffer_store_dwordx2 v[68:69], %[oo1], %[srdO], 0 offen\n\t" \
  ".Lred_end:\n\t"

#define RED_CLOB "v60","v61","v62","v63","v64","v65","v66","v67","v68","v69","v70","v71","v72","v73","v74","v75","v76","v77","v78","v79","v80","v81","v82","v83","v84","v85","v86","v87","v88","v89","v90","v91","v92","v93","v94","v95","v96","v97","v98","v99","v100","v101","v102","v103","v104","v105","v106","v107","v108","v109","v110","v111","v112","v113","v114","v115","v116","v117","v118","v119","v120","v121","v122","v123","v124","v125","v126","v127","v128","v129","v130","v131","v132","v133","v134","v135","v136","v137","v138","v139","v140","v141","v142","v143","v144","v145","v146","v147","v148","v149","v150","v151","v152","v153","v154","v155","v156","v157","v158","v159","v160","v161","v162","v163","v164","v165","v166","v167","v168","v169","v170","v171","v172","v173","v174","v175","v176","v177","v178","v179","v180","v181","v182","v183","v184","v185","v186","v187","v188","v189","v190","v191","s56","s57","s58","s59","s60","s61","s62","s63","s64","s65","s66","s67","s68"


#define GAS __attribute__((address_space(1)))
typedef GAS unsigned long long gu64;
#define RLX_AGENT __ATOMIC_RELAXED, __HIP_MEMORY_SCOPE_AGENT
constexpr unsigned WS_FLAG = 4096;
constexpr unsigned WS_P = 1u << 20;
constexpr unsigned GSTR = 16u * 81920u + 4096u;
DI unsigned rfl(unsigned x) { return __builtin_amdgcn_readfirstlane(x); }
DI __amdgpu_buffer_rsrc_t mk_srd(const void* p, unsigned bytes) {
  return __builtin_amdgcn_make_buffer_rsrc((void*)p, (short)0, (int)bytes, 0x00020000);
}

struct Args { const char* p[2][5]; char* ws; char* out; };

__global__ __launch_bounds__(256, 1) void fused_attn_kernel(const Args a) {
  const unsigned tid = threadIdx.x, lane = tid & 63u;
  unsigned long long tpre;
  unsigned cw;
  {
    unsigned long long pc;
    asm volatile("s_memrealtime %2\n\ts_getpc_b64 %1\n\tglobal_load_dword %0, %3, %1" : "=&v"(cw), "=&s"(pc), "=&s"(tpre) : "v"(((tid >> 6) << 11) | ((tid & 15u) << 7)) : "memory");
    __builtin_amdgcn_sched_barrier(0);
  }
  const unsigned wid = rfl(tid >> 6);
  const unsigned bid = blockIdx.x, xcd = bid & 7u, idx = bid >> 3;
  const unsigned isG = idx & 1u, id2 = idx >> 1;
  const char *xB = a.p[0][0], *kB = a.p[0][1], *vB = a.p[0][2], *wqB = a.p[0][3], *woB = a.p[0][4];
  const char *xA = a.p[1][0], *kA = a.p[1][1], *vA = a.p[1][2], *wqA = a.p[1][3], *woA = a.p[1][4];
  char *ws = a.ws, *out = a.out;
  const unsigned u = wid >> 1, s = wid & 1u;
  const unsigned r16 = lane & 15u, g = lane >> 4;
  const unsigned b = id2 >> 1, prod = 2u * xcd + (id2 & 1u), h = 2u * prod + u;
  const unsigned br = isG ^ 1u;
  unsigned mo = (b * 32u + h) * 262144u + s * 131072u;
  unsigned kvo = isG ? (b * 8u + xcd) * 262144u + s * 131072u : mo;
  const unsigned rotM = ((xcd * 5u + wid * 3u) & 15u) * 8192u;
  unsigned rot = isG ? ((b * 5u + xcd * 3u + s * 7u) & 15u) * 8192u : rotM;
  unsigned qrot = (b + 4u * (id2 & 1u)) & 7u;
  unsigned w8k = wid * 8192u;
  const unsigned xp0 = wid == 3u ? 7u : 3u * wid;
  unsigned fo = WS_FLAG + ((br * 8u + b) * 17u + lane) * 256u + prod * 8u;
  unsigned cto = ((id2 & 3u) * 4u + wid) * 2048u;
  unsigned oW = h * 256u, oX = b * 81920u, oWo = prod * 262144u + wid * 256u, oP = WS_P + (br * 8u + b) * GSTR + prod * 81920u + wid * 20480u;
  unsigned oXt = b * 81920u + (cto & 0x3fffu) * 5u;
  unsigned ko0 = g * 256u + 16u * (r16 ^ g);
  unsigned vo0 = g * 256u + 16u * (r16 ^ (g << 1));
  unsigned wo0 = g * 8192u + 16u * (r16 ^ (g << 1));
  unsigned xo0 = (((lane >> 3) * 1024u + 16u * ((lane & 7u) ^ g)) ^ ((xp0 & 1u) << 6)) + xp0 * 8192u;
  unsigned wov = (lane >> 3) * 1024u + 16u * ((lane & 7u) ^ (g << 1));
  const unsigned q4 = r16 >> 2, p4 = lane & 3u, vrow = 4u * g + q4;
  unsigned wr = u * 16384u + (8u * g + q4) * 256u + 16u * ((8u * s + (p4 >> 1)) ^ ((q4 | ((g & 1u) << 2)) << 1)) + 8u * (p4 & 1u);
  unsigned qw = (g >> 1) * 256u + r16 * 16u + (g & 1u) * 8u;
  unsigned xr = 32768u + r16 * 128u + 16u * (g ^ (r16 >> 1));
  unsigned kb = (256u * r16 + 16u * (g ^ r16)) ^ (s << 7);
  unsigned vb = (256u * vrow + 16u * (((lane >> 1) & 1u) ^ ((vrow & 7u) << 1)) + 8u * (lane & 1u)) ^ (s << 7);
  unsigned gtr = vrow * 128u + 32u * ((vrow >> 1) & 3u) + 8u * p4;
  unsigned l16 = lane * 16u;
  unsigned mrot = rotM | (lane * 128u);
  unsigned owt = prod * 262144u + b * 32768u + w8k + r16 * 512u;
  asm volatile("" : "+s"(mo), "+s"(kvo), "+s"(rot), "+s"(qrot), "+s"(w8k), "+s"(cto), "+s"(oW), "+s"(oX), "+s"(oWo), "+s"(oP), "+s"(oXt),
                    "+v"(ko0), "+v"(vo0), "+v"(wo0), "+v"(xo0), "+v"(wr), "+v"(qw), "+v"(xr), "+v"(kb), "+v"(vb), "+v"(wov), "+v"(gtr), "+v"(l16), "+v"(mrot), "+v"(owt), "+v"(fo));
  asm volatile("s_waitcnt lgkmcnt(0)" : "+s"(tpre), "+s"(xB), "+s"(kB), "+s"(vB), "+s"(wqB), "+s"(woB), "+s"(xA), "+s"(kA), "+s"(vA), "+s"(wqA), "+s"(woA), "+s"(ws), "+s"(out));
  const __amdgpu_buffer_rsrc_t srdK = mk_srd((isG ? kA : kB) + kvo, 131072u);
  const __amdgpu_buffer_rsrc_t srdV = mk_srd((isG ? vA : vB) + kvo, 131072u);
  const __amdgpu_buffer_rsrc_t srdW = mk_srd((isG ? wqA : wqB) + oW, 511u * 8192u + 256u);
  const __amdgpu_buffer_rsrc_t srdX = mk_srd((isG ? xA : xB) + oX, 81920u);
  const __amdgpu_buffer_rsrc_t srdWo = mk_srd((isG ? woA : woB) + oWo, 255u * 1024u + 256u);
  const __amdgpu_buffer_rsrc_t srdP = mk_srd(ws + oP, 20480u);
  const char* const xtb = xB + oXt;
  const char* const mk = kB + mo; const char* const mv = vB + mo;
  const char* const wtp = ((lane & 16u) ? woB : woA) + owt;
  const unsigned long long tag = __hip_atomic_load((gu64*)ws, RLX_AGENT) + 1ull;

  asm volatile("s_nop 4\n\t" ATTN_ASM
    :: [kb] "v"(kb), [vb] "v"(vb), [ko0] "v"(ko0), [vo0] "v"(vo0), [wo0] "v"(wo0), [xo0] "v"(xo0), [qw] "v"(qw),
       [wr] "v"(wr), [xr] "v"(xr), [wov] "v"(wov), [gtr] "v"(gtr), [l16] "v"(l16),
       [srdK] "s"(srdK), [srdV] "s"(srdV), [srdW] "s"(srdW), [srdX] "s"(srdX), [srdWo] "s"(srdWo), [srdP] "s"(srdP),
       [qrot] "s"(qrot), [rot] "s"(rot), [w8k] "s"(w8k), [isg] "s"(isG), [xtb] "s"(xtb), [cto] "s"(cto), [wtp] "v"(wtp), [mk] "s"(mk), [mv] "s"(mv), [mrot] "v"(mrot), [cw] "v"(cw), [tpre] "s"((unsigned)tpre)
    : "memory", "m0", "vcc", "scc", ATTN_CLOB);

  asm volatile("s_waitcnt vmcnt(0)\n\ts_barrier" ::: "memory");
  if (tid < 17u) __hip_atomic_store((gu64*)(ws + fo), tag, RLX_AGENT);

  {
    const unsigned oo0 = (wid * 16u + r16) * 2048u + 8u * g;
    const unsigned oo1 = g < 2u ? (64u + r16) * 2048u + 8u * wid + 32u * g : 0x7fffffffu;
    const unsigned one = (r16 & 1u) ? 0x3f800000u : 0x00003f80u;
    const unsigned m4 = (g == q4) ? one : 0u;
    const u32x4 slo = {(r16 & 2u) ? 0u : m4, (r16 & 2u) ? m4 : 0u, 0u, 0u};
    const u32x4 shi = {0u, 0u, slo[0], slo[1]};
    const unsigned h2 = r16 >> 1;
    const u32x4 sfi = {h2 == 0u ? one : 0u, h2 == 1u ? one : 0u, h2 == 2u ? one : 0u, h2 == 3u ? one : 0u};
    gu64* fp = (gu64*)(ws + WS_FLAG + ((br * 8u + b) * 17u + prod) * 256u + r16 * 8u);
    const __amdgpu_buffer_rsrc_t srdR = mk_srd(ws + WS_P + (br * 8u + b) * GSTR + prod * 5120u, 16u * 81920u - prod * 5120u);
    const __amdgpu_buffer_rsrc_t srdO = mk_srd(out + (b * 81920u + br * 512u + prod * 32u) * 2u, 163840u - (br * 512u + prod * 32u) * 2u);
    asm volatile("s_nop 4\n\t" RED_ASM
                 :: [fp] "v"(fp), [tag] "v"(tag), [srdR] "s"(srdR), [srdO] "s"(srdO), [l16] "v"(l16), [e16] "v"(g * 81920u + r16 * 16u),
                    [so] "s"(wid * 1024u), [se] "s"(4096u + wid * 256u), [oo0] "v"(oo0), [oo1] "v"(oo1), [slo] "v"(slo), [shi] "v"(shi), [sfi] "v"(sfi)
                 : "memory", "vcc", "scc", RED_CLOB);
  }
  if (bid == 8u && wid == 3u) {
    for (unsigned spins = 0; spins < (1u << 16); ++spins) {
      bool ok = true;
#pragma unroll
      for (int i = 0; i < 4; ++i) ok &= __hip_atomic_load((gu64*)(ws + WS_FLAG + ((4u * i + g) * 17u + 16u) * 256u + r16 * 8u), RLX_AGENT) == tag;
      if (__all(ok)) break;
      __builtin_amdgcn_s_sleep(8);
    }
    if (lane == 0) __hip_atomic_store((gu64*)ws, tag, RLX_AGENT);
  }
}

extern "C" void kernel_launch(void* const* d_in, const int* in_sizes, int n_in, void* d_out, int out_size, void* d_ws, size_t ws_size, hipStream_t stream) {
  (void)hipFuncSetAttribute((const void*)fused_attn_kernel, hipFuncAttributeMaxDynamicSharedMemorySize, 163840);
  Args a;
  for (int i = 0; i < 5; ++i) { a.p[0][i] = (const char*)d_in[5 + i]; a.p[1][i] = (const char*)d_in[i]; }
  a.ws = (char*)d_ws; a.out = (char*)d_out;
  fused_attn_kernel<<<256, 256, 163840, stream>>>(a);
}
```
